```python
import torch

def _softmax(x, axis=-1):
    return torch.softmax(x, dim=axis)
def _gelu(x):
    return torch.nn.functional.gelu(x, approximate="tanh")
def _silu(x):
    return torch.nn.functional.silu(x)
def _sigmoid(x):
    return torch.sigmoid(x)
def _layernorm(x, axis=-1, eps=1e-5):
    m = x.mean(dim=axis, keepdim=True)
    v = x.var(dim=axis, keepdim=True, unbiased=False)
    return (x - m) / torch.sqrt(v + eps)
def _scatter_add(tgt, idx, src, axis=0):
    return tgt.clone().index_add_(axis, idx, src)
def _segment_sum(src, seg, n):
    out = torch.zeros((n,) + src.shape[1:], dtype=src.dtype)
    return out.index_add_(0, seg, src)
def _pad(x, pw):
    flat = tuple(v for pair in reversed(pw) for v in pair)
    return torch.nn.functional.pad(x, flat)

kernel_name = 'sequential_preLN__gqa_block_cached__geglu_ffn__decode'

def setup_inputs():

    _w = (lambda a: (lambda t: t.to(torch.bfloat16) if t.is_floating_point() else t)(torch.as_tensor(a)))
    def _A_setup():
        B, Sq, D = 8, 1, 512
        Hq, Hkv, Sk, Dh = 32, 8, 8192, 128
        assert Hq % Hkv == 0, (Hq, Hkv)
        return {
            "x": _w((lambda *s: torch.randn(*s, dtype=torch.bfloat16) * 0.05)(B, Sq, D)),
            "k_cache": _w((lambda *s: torch.randn(*s, dtype=torch.bfloat16) * 3.2)(B, Hkv, Sk, Dh)),
            "v_cache": _w((lambda *s: torch.randn(*s, dtype=torch.bfloat16) * 0.4)(B, Hkv, Sk, Dh)),
            "wq": _w((lambda *s: torch.randn(*s, dtype=torch.bfloat16) * 3.2)(D, Hq * Dh) * (D ** -0.5)),
            "wo": _w((lambda *s: torch.randn(*s, dtype=torch.bfloat16) * 0.4)(Hq * Dh, D) * ((Hq * Dh) ** -0.5)),
        }
    _A_in = _A_setup()

    def _B_setup():
        B, S, D, F = 8, 1, 512, 1792
        return {
            "x": _w((lambda *s: torch.randn(*s, dtype=torch.bfloat16) * 0.05)(B, S, D)),
            "w_gate": _w((lambda *s: torch.randn(*s, dtype=torch.bfloat16) * 0.05)(D, F)),
            "w_up": _w((lambda *s: torch.randn(*s, dtype=torch.bfloat16) * 0.05)(D, F)),
            "w_down": _w((lambda *s: torch.randn(*s, dtype=torch.bfloat16) * 0.05)(F, D)),
        }
    _B_in = _B_setup()

    return {
        "A_x": _A_in["x"],
        "A_k_cache": _A_in["k_cache"],
        "A_v_cache": _A_in["v_cache"],
        "A_wq": _A_in["wq"],
        "A_wo": _A_in["wo"],
        "B_x": _B_in["x"],
        "B_w_gate": _B_in["w_gate"],
        "B_w_up": _B_in["w_up"],
        "B_w_down": _B_in["w_down"],
    }

def reference(A_x, A_k_cache, A_v_cache, A_wq, A_wo, B_x, B_w_gate, B_w_up, B_w_down):
    def _A_ref(x, k_cache, v_cache, wq, wo):
        B, Sq, D = x.shape
        Hq, Dh = 32, 128
        Hkv = k_cache.shape[1]
        rep = Hq // Hkv
        q = (lambda x, *d: x.permute(*d))((x @ wq).reshape(B, Sq, Hq, Dh), 0, 2, 1, 3)
        k = (lambda x, n, axis: torch.repeat_interleave(x, n, dim=axis))(k_cache, rep, axis=1)
        v = (lambda x, n, axis: torch.repeat_interleave(x, n, dim=axis))(v_cache, rep, axis=1)
        scores = torch.einsum("bhqd,bhkd->bhqk", q, k) * (Dh ** -0.5)
        p = _softmax(scores, axis=-1)
        o = torch.einsum("bhqk,bhkd->bhqd", p, v)
        return (lambda x, *d: x.permute(*d))(o, 0, 2, 1, 3).reshape(B, Sq, Hq * Dh) @ wo

    def _B_ref(x, w_gate, w_up, w_down):
        return (_gelu(x @ w_gate) * (x @ w_up)) @ w_down

    x = A_x
    h = x + _A_ref(_layernorm(x, axis=-1), A_k_cache, A_v_cache, A_wq, A_wo)
    h2 = h + _B_ref(_layernorm(h, axis=-1), B_w_gate, B_w_up, B_w_down)
    return h2
```

```cpp
#include <hip/hip_runtime.h>
#include <hip/hip_bf16.h>
#include <stdint.h>

typedef unsigned short u16;
using u32x4  = __attribute__((ext_vector_type(4))) unsigned;
using f32x4  = __attribute__((ext_vector_type(4))) float;
using bf16x2 = __attribute__((ext_vector_type(2))) __bf16;

constexpr int B = 8, D = 512, HQ = 32, HKV = 8, SK = 8192, DH = 128, F = 1792, G = 4, NSPLIT = 4;

__device__ __forceinline__ float bf2f(u16 v) { return __uint_as_float((unsigned)v << 16); }
__device__ __forceinline__ u16 f2bf(float f) { unsigned u = __float_as_uint(f); u += 0x7FFFu + ((u >> 16) & 1u); return (u16)(u >> 16); }
__device__ __forceinline__ float rbf(float f) { return bf2f(f2bf(f)); }
__device__ __forceinline__ float dot2(unsigned k, unsigned q, float acc) {
  return __builtin_amdgcn_fdot2_f32_bf16(__builtin_bit_cast(bf16x2, k), __builtin_bit_cast(bf16x2, q), acc, false);
}
__device__ __forceinline__ float wave_sum(float v) { for (int m = 32; m >= 1; m >>= 1) v += __shfl_xor(v, m); return v; }
__device__ __forceinline__ float wave_max(float v) { for (int m = 32; m >= 1; m >>= 1) v = fmaxf(v, __shfl_xor(v, m)); return v; }

__device__ void ln_rows(const float* in, float* out, int tid) {
  const int wave = tid >> 6, lane = tid & 63;
  for (int r = wave; r < B; r += 4) {
    float v[8]; float s = 0.f;
    for (int j = 0; j < 8; ++j) { v[j] = in[r * D + lane + 64 * j]; s += v[j]; }
    s = wave_sum(s); const float m = s * (1.f / D);
    float q = 0.f; for (int j = 0; j < 8; ++j) { const float d = v[j] - m; q += d * d; }
    q = wave_sum(q); const float var = q * (1.f / D);
    const float inv = 1.f / sqrtf(var + 1e-5f);
    for (int j = 0; j < 8; ++j) out[r * D + lane + 64 * j] = rbf((v[j] - m) * inv);
  }
}

__global__ void __launch_bounds__(256) k_q(const u16* x, const u16* wq, u16* q) {
  __shared__ float xs[B * D], xn[B * D], red[16][16][8];
  const int tid = threadIdx.x;
  for (int i = tid; i < B * D; i += 256) xs[i] = bf2f(x[i]);
  __syncthreads(); ln_rows(xs, xn, tid); __syncthreads();
  const int col = blockIdx.x * 16 + (tid & 15), ks = tid >> 4;
  float acc[8] = {0};
  for (int k = ks * 32; k < ks * 32 + 32; ++k) { const float w = bf2f(wq[(size_t)k * (HQ * DH) + col]); for (int b = 0; b < 8; ++b) acc[b] = fmaf(xn[b * D + k], w, acc[b]); }
  for (int b = 0; b < 8; ++b) red[ks][tid & 15][b] = acc[b];
  __syncthreads();
  if (tid < 128) { const int c = tid & 15, b = tid >> 4; float s = 0.f; for (int k = 0; k < 16; ++k) s += red[k][c][b]; q[b * (HQ * DH) + blockIdx.x * 16 + c] = f2bf(s); }
}

__global__ void __launch_bounds__(256) k_attn(const u16* q, const u16* K, const u16* V, float* pml, float* po) {
  __shared__ float sp[4][64];
  __shared__ float mrg[4][G][DH + 4];
  const int s = blockIdx.x, kvh = blockIdx.y, b = blockIdx.z;
  const int tid = threadIdx.x, lane = tid & 63, wave = tid >> 6, r = lane >> 4, c = lane & 15;
  u32x4 qp[G];
  for (int g = 0; g < G; ++g) qp[g] = *(const u32x4*)(q + (size_t)b * HQ * DH + (kvh * G + g) * DH + 8 * c);
  const size_t base = ((size_t)(b * HKV + kvh) * SK + (size_t)s * (SK / NSPLIT)) * DH;
  float m[G], l[G], acc[G][8];
  for (int g = 0; g < G; ++g) { m[g] = -INFINITY; l[g] = 0.f; for (int k = 0; k < 8; ++k) acc[g][k] = 0.f; }
  for (int t0 = wave * 16; t0 < SK / NSPLIT; t0 += 64) {
    u32x4 Kc[4], Vc[4];
    for (int t = 0; t < 4; ++t) { Kc[t] = __builtin_nontemporal_load((const u32x4*)(K + base + (size_t)(t0 + r + 4 * t) * DH + 8 * c)); Vc[t] = __builtin_nontemporal_load((const u32x4*)(V + base + (size_t)(t0 + r + 4 * t) * DH + 8 * c)); }
    float x[4][G];
    for (int t = 0; t < 4; ++t) for (int g = 0; g < G; ++g) { float d = 0.f; for (int k = 0; k < 4; ++k) d = dot2(Kc[t][k], qp[g][k], d);
      for (int o = 1; o < 16; o <<= 1) d += __shfl_xor(d, o); x[t][g] = rbf(rbf(d) * 0.08838834764831845f); }
    for (int g = 0; g < G; ++g) {
      float pm = fmaxf(fmaxf(x[0][g], x[1][g]), fmaxf(x[2][g], x[3][g]));
      pm = fmaxf(pm, __shfl_xor(pm, 16)); pm = fmaxf(pm, __shfl_xor(pm, 32));
      const float mn = fmaxf(m[g], pm), al = __expf(m[g] - mn);
      float ps = 0.f; float p[4];
      for (int t = 0; t < 4; ++t) { p[t] = __expf(x[t][g] - mn); ps += p[t]; }
      ps += __shfl_xor(ps, 16); ps += __shfl_xor(ps, 32);
      l[g] = l[g] * al + ps; m[g] = mn;
      for (int k = 0; k < 8; ++k) acc[g][k] *= al;
      for (int t = 0; t < 4; ++t) for (int k = 0; k < 4; ++k) { acc[g][2 * k] = fmaf(p[t], __uint_as_float(Vc[t][k] << 16), acc[g][2 * k]); acc[g][2 * k + 1] = fmaf(p[t], __uint_as_float(Vc[t][k] & 0xffff0000u), acc[g][2 * k + 1]); }
    }
  }
  for (int g = 0; g < G; ++g) for (int k = 0; k < 8; ++k) { float v = acc[g][k]; v += __shfl_xor(v, 16); v += __shfl_xor(v, 32); acc[g][k] = v; }
  if (r == 0) { for (int g = 0; g < G; ++g) { for (int k = 0; k < 8; ++k) mrg[wave][g][4 + 8 * c + k] = acc[g][k]; if (c == 0) { mrg[wave][g][0] = m[g]; mrg[wave][g][1] = l[g]; } } }
  __syncthreads();
  { const int g = wave; float M = -INFINITY; for (int w = 0; w < 4; ++w) M = fmaxf(M, mrg[w][g][0]);
    float L = 0.f, o0 = 0.f, o1 = 0.f; for (int w = 0; w < 4; ++w) { const float wt = __expf(mrg[w][g][0] - M); L = fmaf(wt, mrg[w][g][1], L); o0 = fmaf(wt, mrg[w][g][4 + 2 * lane], o0); o1 = fmaf(wt, mrg[w][g][5 + 2 * lane], o1); }
    const size_t slot = ((size_t)(b * HQ + kvh * G + g)) * NSPLIT + s;
    po[slot * DH + 2 * lane] = o0; po[slot * DH + 2 * lane + 1] = o1; if (lane == 0) { pml[2 * slot] = M; pml[2 * slot + 1] = L; } }
  (void)sp;
}

__global__ void __launch_bounds__(256) k_wo(const u16* x, const float* pml, const float* po, const u16* wo, float* h) {
  __shared__ float o[B][HQ * DH / 8];
  __shared__ float red[32][8][8];
  const int tid = threadIdx.x, col = blockIdx.x * 8 + (tid & 7), ks = tid >> 3;
  float acc[8] = {0};
  for (int ch = 0; ch < 8; ++ch) {
    __syncthreads();
    for (int i = tid; i < B * 512; i += 256) { const int b = i >> 9, kk = i & 511, hq = ch * 4 + (kk >> 7), d = kk & 127; const size_t row = (size_t)(b * HQ + hq) * NSPLIT;
      float M = -INFINITY; for (int s = 0; s < NSPLIT; ++s) M = fmaxf(M, pml[2 * (row + s)]);
      float L = 0.f, a = 0.f; for (int s = 0; s < NSPLIT; ++s) { const float w = __expf(pml[2 * (row + s)] - M); L = fmaf(w, pml[2 * (row + s) + 1], L); a = fmaf(w, po[(row + s) * DH + d], a); }
      o[b][kk] = rbf(a / L); }
    __syncthreads();
    for (int k = ks * 16; k < ks * 16 + 16; ++k) { const float w = bf2f(wo[(size_t)(ch * 512 + k) * D + col]); for (int b = 0; b < 8; ++b) acc[b] = fmaf(o[b][k], w, acc[b]); }
  }
  for (int b = 0; b < 8; ++b) red[ks][tid & 7][b] = acc[b];
  __syncthreads();
  if (tid < 64) { const int c = tid & 7, b = tid >> 3; float s = 0.f; for (int k = 0; k < 32; ++k) s += red[k][c][b]; const int cc = blockIdx.x * 8 + c; h[b * D + cc] = rbf(bf2f(x[b * D + cc]) + rbf(s)); }
}

__global__ void __launch_bounds__(256) k_gu(const float* h, const u16* wg, const u16* wu, float* g) {
  __shared__ float xs[B * D], xn[B * D], red[2][16][16][8];
  const int tid = threadIdx.x;
  for (int i = tid; i < B * D; i += 256) xs[i] = h[i];
  __syncthreads(); ln_rows(xs, xn, tid); __syncthreads();
  const int col = blockIdx.x * 16 + (tid & 15), ks = tid >> 4;
  float a0[8] = {0}, a1[8] = {0};
  for (int k = ks * 32; k < ks * 32 + 32; ++k) { const float w0 = bf2f(wg[(size_t)k * F + col]), w1 = bf2f(wu[(size_t)k * F + col]); for (int b = 0; b < 8; ++b) { a0[b] = fmaf(xn[b * D + k], w0, a0[b]); a1[b] = fmaf(xn[b * D + k], w1, a1[b]); } }
  for (int b = 0; b < 8; ++b) { red[0][ks][tid & 15][b] = a0[b]; red[1][ks][tid & 15][b] = a1[b]; }
  __syncthreads();
  if (tid < 128) { const int c = tid & 15, b = tid >> 4; float s0 = 0.f, s1 = 0.f; for (int k = 0; k < 16; ++k) { s0 += red[0][k][c][b]; s1 += red[1][k][c][b]; }
    s0 = rbf(s0); s1 = rbf(s1);
    const float u = 0.7978845608028654f * (s0 + 0.044715f * s0 * s0 * s0); const float t = 1.f - 2.f / (__expf(2.f * u) + 1.f);
    const float ge = rbf(0.5f * s0 * (1.f + t)); g[b * F + blockIdx.x * 16 + c] = rbf(ge * s1); }
}

__global__ void __launch_bounds__(256) k_down(const float* h, const float* g, const u16* wd, u16* out) {
  __shared__ float gs[B * F], red[32][8][8];
  const int tid = threadIdx.x, col = blockIdx.x * 8 + (tid & 7), ks = tid >> 3;
  for (int i = tid; i < B * F; i += 256) gs[i] = g[i];
  __syncthreads();
  float acc[8] = {0};
  for (int k = ks * 56; k < ks * 56 + 56; ++k) { const float w = bf2f(wd[(size_t)k * D + col]); for (int b = 0; b < 8; ++b) acc[b] = fmaf(gs[b * F + k], w, acc[b]); }
  for (int b = 0; b < 8; ++b) red[ks][tid & 7][b] = acc[b];
  __syncthreads();
  if (tid < 64) { const int c = tid & 7, b = tid >> 3; float s = 0.f; for (int k = 0; k < 32; ++k) s += red[k][c][b]; const int cc = blockIdx.x * 8 + c; out[b * D + cc] = f2bf(h[b * D + cc] + rbf(s)); }
}

extern "C" void kernel_launch(void* const* d_in, const int* in_sizes, int n_in, void* d_out, int out_size, void* d_ws, size_t ws_size, hipStream_t stream) {
  const u16* x = (const u16*)d_in[0]; const u16* K = (const u16*)d_in[1]; const u16* V = (const u16*)d_in[2]; const u16* wq = (const u16*)d_in[3]; const u16* wo = (const u16*)d_in[4];
  const u16* wg = (const u16*)d_in[6]; const u16* wu = (const u16*)d_in[7]; const u16* wd = (const u16*)d_in[8];
  char* ws = (char*)d_ws;
  u16* q = (u16*)ws; float* pml = (float*)(ws + (1 << 20)); float* po = (float*)(ws + (2 << 20)); float* h = (float*)(ws + (4 << 20)); float* g = (float*)(ws + (5 << 20));
  k_q<<<256, 256, 0, stream>>>(x, wq, q);
  k_attn<<<dim3(NSPLIT, HKV, B), 256, 0, stream>>>(q, K, V, pml, po);
  k_wo<<<64, 256, 0, stream>>>(x, pml, po, wo, h);
  k_gu<<<112, 256, 0, stream>>>(h, wg, wu, g);
  k_down<<<64, 256, 0, stream>>>(h, g, wd, (u16*)d_out);
}
```

```cpp
#include <hip/hip_runtime.h>
#include <hip/hip_bf16.h>
#include <stdint.h>

typedef unsigned short u16;
typedef unsigned u32;
typedef unsigned long long u64;
typedef u32 u32x4 __attribute__((ext_vector_type(4)));
typedef u32 u32x2 __attribute__((ext_vector_type(2)));
typedef float f32x4 __attribute__((ext_vector_type(4)));
typedef float f32x2 __attribute__((ext_vector_type(2)));
typedef short s16x8 __attribute__((ext_vector_type(8)));
typedef short s16x4 __attribute__((ext_vector_type(4)));
typedef __bf16 bf16x8_t __attribute__((ext_vector_type(8)));
#define GAS __attribute__((address_space(1)))
#define LAS __attribute__((address_space(3)))
#define RLX __ATOMIC_RELAXED, __HIP_MEMORY_SCOPE_AGENT

#define QPOS 0
#define HOLD 1
#define PASSES 1
#define SLEEP0 8
#define QSLEEP 4
#define LATEW 1
#define STAMPS 0

constexpr int NB = 8, DM = 512, HQ = 32, HKV = 8, SK = 8192, DH = 128, FF = 1792;
constexpr int NWG = 256, NTHR = 512;
constexpr int NGU = FF / 32;
constexpr int TOK_WG = SK / 4;
constexpr int NIT = TOK_WG / 16 / 8;
constexpr float SC_LOG2 = 0.08838834764831845f * 1.4426950408889634f;
constexpr float LN_EPS = 1e-5f;
constexpr int AUX_NT = 2, AUX_SC1 = 16;
constexpr int SPIN_MAX = 1 << 16;

constexpr int FS = 256;
constexpr int WS_E    = 0;
constexpr int WS_H1F  = 4096;
constexpr int WS_H2F  = WS_H1F + 256 * FS;
constexpr int WS_H3F  = WS_H2F + 64 * FS;
constexpr int WS_QG   = WS_H3F + 64 * FS;
constexpr int WS_PART = WS_QG + HKV * NB * 4 * 512 * 8;
constexpr int WS_Y    = WS_PART + HKV * 32 * 4 * 128 * 2;
constexpr int WS_G    = WS_Y + HKV * NB * DM * 2;
constexpr int WS_END  = WS_G + NB * FF * 2;
constexpr int WS_STAMP = 2 << 20;

constexpr int L_ROLE = 0;
constexpr int L_WQ = 0;
constexpr int L_MRG = 65536;
constexpr int A_STR = 1040;
constexpr int L_A = L_MRG + 16384;
constexpr int L_RED = L_A + 8 * A_STR;
constexpr int L_Q = L_RED + 8192;
constexpr int L_ST = L_Q + 1024;
constexpr int L_LAM = L_ST + 256;
constexpr int P_STR = 1040;
constexpr int L_P = L_LAM + 512;
constexpr int G_STR = 3600;
constexpr int LDS_BYTES = L_P + 32 * P_STR;

typedef __bf16 bf16x2_t __attribute__((ext_vector_type(2)));
__device__ __forceinline__ u32 cvtpk(float lo, float hi) { const bf16x2_t r = {(__bf16)lo, (__bf16)hi}; return __builtin_bit_cast(u32, r); }
__device__ __forceinline__ float u2f(u32 v) { return __builtin_bit_cast(float, v); }
__device__ __forceinline__ u32 f2u(float v) { return __builtin_bit_cast(u32, v); }
__device__ __forceinline__ float bflo(u32 v) { return __builtin_bit_cast(float, v << 16); }
__device__ __forceinline__ float bfhi(u32 v) { return __builtin_bit_cast(float, v & 0xffff0000u); }
template <int CTRL> __device__ __forceinline__ float dpp(float x) { return __builtin_bit_cast(float, __builtin_amdgcn_mov_dpp(__builtin_bit_cast(int, x), CTRL, 0xf, 0xf, true)); }
__device__ __forceinline__ float xrow_max(float x) {
  auto s = __builtin_amdgcn_permlane16_swap(f2u(x), f2u(x), false, false);
  x = fmaxf(u2f(s[0]), u2f(s[1]));
  auto t = __builtin_amdgcn_permlane32_swap(f2u(x), f2u(x), false, false);
  return fmaxf(u2f(t[0]), u2f(t[1]));
}
__device__ __forceinline__ float xrow_sum(float x) {
  auto s = __builtin_amdgcn_permlane16_swap(f2u(x), f2u(x), false, false);
  x = u2f(s[0]) + u2f(s[1]);
  auto t = __builtin_amdgcn_permlane32_swap(f2u(x), f2u(x), false, false);
  return u2f(t[0]) + u2f(t[1]);
}
__device__ __forceinline__ float wave_sum(float x) {
  x += dpp<0xB1>(x); x += dpp<0x4E>(x); x += dpp<0x141>(x); x += dpp<0x140>(x);
  return xrow_sum(x);
}
__device__ __forceinline__ f32x4 mfma32(u32x4 a, u32x4 b, f32x4 c) {
  return __builtin_amdgcn_mfma_f32_16x16x32_bf16(__builtin_bit_cast(bf16x8_t, a), __builtin_bit_cast(bf16x8_t, b), c, 0, 0, 0);
}
__device__ __forceinline__ f32x4 mfma16(u32x2 a, u32x2 b, f32x4 c) {
  return __builtin_amdgcn_mfma_f32_16x16x16bf16_1k(__builtin_bit_cast(s16x4, a), __builtin_bit_cast(s16x4, b), c, 0, 0, 0);
}
typedef short v4i16_t __attribute__((ext_vector_type(4)));
__device__ __forceinline__ u32x2 tr_read(LAS unsigned char* p) {
  return __builtin_bit_cast(u32x2, __builtin_amdgcn_ds_read_tr16_b64_v4i16((LAS v4i16_t*)p));
}
__device__ __forceinline__ u32x4 bfrag(LAS unsigned char* p, int second_off) {
  const u32x2 a = tr_read(p), b = tr_read(p + second_off);
  return u32x4{a.x, a.y, b.x, b.y};
}
__device__ __forceinline__ __amdgpu_buffer_rsrc_t mkrs(const void* p, u32 bytes) { return __builtin_amdgcn_make_buffer_rsrc((void*)p, 0, bytes, 0x00020000); }
#define LDB128(rs, vo, so, aux) __builtin_amdgcn_raw_buffer_load_b128(rs, vo, so, aux)
#define SB() __builtin_amdgcn_sched_barrier(0)
#define DRAIN() asm volatile("s_waitcnt vmcnt(0)" ::: "memory")
#define MEMFENCE() asm volatile("" ::: "memory")
#define RAWBAR() asm volatile("s_barrier" ::: "memory")
__device__ __forceinline__ void wg_barrier() { asm volatile("s_waitcnt lgkmcnt(0)\n\ts_barrier" ::: "memory"); }

__device__ __forceinline__ u32x4 ln_row(const float (&v)[8]) {
  float s = ((v[0] + v[1]) + (v[2] + v[3])) + ((v[4] + v[5]) + (v[6] + v[7]));
  const float mean = wave_sum(s) * (1.0f / DM);
  float d[8], q = 0.f;
#pragma unroll
  for (int i = 0; i < 8; ++i) { d[i] = v[i] - mean; q = fmaf(d[i], d[i], q); }
  const float var = wave_sum(q) * (1.0f / DM);
  const float rs = __builtin_amdgcn_rsqf(var + LN_EPS);
  return u32x4{cvtpk(d[0] * rs, d[1] * rs), cvtpk(d[2] * rs, d[3] * rs), cvtpk(d[4] * rs, d[5] * rs), cvtpk(d[6] * rs, d[7] * rs)};
}

#define STAMP(i) do {} while (0)
#define STAMP_FLUSH() do {} while (0)

__global__ void __launch_bounds__(NTHR, 2)
fused_decode_block(const u16* __restrict__ x, const u16* __restrict__ Kc, const u16* __restrict__ Vc, const u16* __restrict__ wq, const u16* __restrict__ wo,
                   const u16* __restrict__ wg, const u16* __restrict__ wu, const u16* __restrict__ wd, u16* __restrict__ out, unsigned char* __restrict__ ws) {
  __shared__ __attribute__((aligned(16))) unsigned char lds_raw[LDS_BYTES];
  LAS unsigned char* L = (LAS unsigned char*)lds_raw;
  const int tid = threadIdx.x, lane = tid & 63, w = __builtin_amdgcn_readfirstlane(tid >> 6);
  const int blk = blockIdx.x, kvh = blk & 7, j = blk >> 3, b = j >> 2, sp = j & 3;
  const int g = lane >> 4, c16 = lane & 15;
  GAS unsigned char* wsg = (GAS unsigned char*)ws;
  STAMP(0);

  u64 E;
  asm volatile("s_load_dwordx2 %0, %1, 0x0" : "=s"(E) : "s"(ws));
  const __amdgpu_buffer_rsrc_t rx = mkrs(x, NB * DM * 2);
  const __amdgpu_buffer_rsrc_t rwq = mkrs(wq, DM * HQ * DH * 2);
  const size_t kvbase = ((size_t)(b * HKV + kvh) * SK + (size_t)sp * TOK_WG) * DH;
  const __amdgpu_buffer_rsrc_t rk = mkrs(Kc + kvbase, TOK_WG * DH * 2);
  const __amdgpu_buffer_rsrc_t rv = mkrs(Vc + kvbase, TOK_WG * DH * 2);
  const __amdgpu_buffer_rsrc_t rws = mkrs(ws, WS_END);

  const int trl = (8 * g + ((lane >> 2) & 3)) * 32 + (lane & 3) * 8;
  const int jj = 3 * b + sp - 1;
  const int role = sp == 0 ? 1 : (jj < 7 ? 2 : (jj < 11 ? 3 : 0));
  const int cgu = kvh * 7 + jj, cdn = kvh * 4 + (jj - 7);
#define LOAD_WEIGHTS(wst) do { \
      const u16* p0 = role == 1 ? wo : (role == 2 ? wg : wd); \
      const u16* p1 = role == 2 ? wu : p0; \
      const u32 n0b = role == 0 ? 0u : (role == 1 ? (u32)(HQ * DH * DM * 2) : (u32)(DM * FF * 2)); \
      const u32 n1b = role == 3 ? 0u : n0b; \
      const __amdgpu_buffer_rsrc_t r0 = mkrs(p0, n0b), r1 = mkrs(p1, n0b), r2 = mkrs(p1, n1b); \
      const int v0o = role == 1 ? ((kvh * 512 + 128 * w + (lane >> 3)) * DM + 64 * b + 8 * (lane & 7)) * 2 \
                    : role == 2 ? ((128 * w + (lane >> 2)) * FF + 32 * cgu + 8 * (lane & 3)) * 2 \
                                : ((448 * w + (lane >> 1)) * DM + 16 * cdn + 8 * (lane & 1)) * 2; \
      const int st = role == 1 ? 8 * DM * 2 : role == 2 ? 16 * FF * 2 : 32 * DM * 2; \
      const int v8o = role == 2 ? v0o : v0o + 8 * st; \
      _Pragma("unroll") for (int i = 0; i < 8; ++i) wst[i] = LDB128(r0, v0o + i * st, 0, AUX_NT); \
      _Pragma("unroll") for (int i = 0; i < 6; ++i) wst[8 + i] = LDB128(r1, v8o + i * st, 0, AUX_NT); \
      _Pragma("unroll") for (int i = 6; i < 8; ++i) wst[8 + i] = LDB128(r2, v8o + i * st, 0, AUX_NT); \
      SB(); } while (0)
#define STAGE_WEIGHTS(wst) do { \
    if (role == 1) { \
      _Pragma("unroll") for (int i = 0; i < 16; ++i) { const int r = 128 * w + 8 * i + (lane >> 3), ch = lane & 7; \
        *(LAS u32x4*)(L + L_ROLE + (r ^ ((r >> 2) & 1)) * 128 + 32 * ((ch >> 1) ^ (((r >> 3) & 1) | (((r >> 5) & 1) << 1))) + 16 * (ch & 1)) = wst[i]; } \
    } else if (role == 2) { \
      _Pragma("unroll") for (int i = 0; i < 16; ++i) { const int r = 128 * w + 16 * (i & 7) + (lane >> 2), ch = lane & 3; \
        *(LAS u32x4*)(L + L_ROLE + (i >> 3) * 32768 + r * 64 + 32 * ((ch >> 1) ^ ((r >> 3) & 1)) + 16 * (ch & 1)) = wst[i]; } \
    } else if (role == 3) { \
      _Pragma("unroll") for (int i = 0; i < 14; ++i) *(LAS u32x4*)(L + L_ROLE + (448 * w + 32 * i) * 32 + lane * 16) = wst[i]; \
    } } while (0)
  u64 T64;
  if (w < 4) {
    const u32x4 xr0 = LDB128(rx, (2 * w * DM + 8 * lane) * 2, 0, 0), xr1 = LDB128(rx, ((2 * w + 1) * DM + 8 * lane) * 2, 0, 0);
    u32x4 wqr[4];
#pragma unroll
    for (int i = 0; i < 4; ++i) wqr[i] = LDB128(rwq, ((128 * sp + 32 * w + 8 * i + (lane >> 3)) * (HQ * DH) + kvh * 512 + 64 * b + 8 * (lane & 7)) * 2, 0, AUX_NT);
    SB(); RAWBAR(); SB();
    float v0[8], v1[8];
#pragma unroll
    for (int i = 0; i < 4; ++i) { v0[2 * i] = bflo(xr0[i]); v0[2 * i + 1] = bfhi(xr0[i]); v1[2 * i] = bflo(xr1[i]); v1[2 * i + 1] = bfhi(xr1[i]); }
    const u32x4 n0 = ln_row(v0), n1 = ln_row(v1);
    *(LAS u32x4*)(L + L_A + 2 * w * A_STR + lane * 16) = n0;
    *(LAS u32x4*)(L + L_A + (2 * w + 1) * A_STR + lane * 16) = n1;
#pragma unroll
    for (int i = 0; i < 4; ++i) *(LAS u32x4*)(L + L_WQ + (32 * w + 8 * i + (lane >> 3)) * 144 + (lane & 7) * 16) = wqr[i];
    STAMP(12);
    wg_barrier();
    STAMP(13);
    const u32x4 a = *(LAS u32x4*)(L + L_A + (lane & 7) * A_STR + (128 * sp + 32 * w + 8 * g) * 2);
#pragma unroll
    for (int nt = 0; nt < 4; ++nt) {
      LAS unsigned char* p = L + L_WQ + (32 * w + 8 * g + ((lane >> 2) & 3)) * 144 + nt * 32 + (lane & 3) * 8;
      const u32x2 lo = tr_read(p), hi = tr_read(p + 4 * 144);
      const f32x4 acc = mfma32(a, u32x4{lo.x, lo.y, hi.x, hi.y}, f32x4{0.f, 0.f, 0.f, 0.f});
      if (lane < 32) {
#pragma unroll
        for (int r = 0; r < 4; ++r) *(LAS float*)(L + L_P + ((w * 8 + 4 * g + r) * 64 + 16 * nt + c16) * 4) = acc[r];
      }
    }
    wg_barrier();
    STAMP(14);
    asm volatile("s_waitcnt lgkmcnt(0)" : "+s"(E));
    T64 = E + 1;
    const u64 T40 = T64 & 0xFFFFFFFFFFull;
#pragma unroll
    for (int i = 0; i < 2; ++i) {
      const int o = tid + 256 * i;
      float sum = 0.f;
#pragma unroll
      for (int ww = 0; ww < 4; ++ww) sum += *(LAS float*)(L + L_P + (ww * 512 + o) * 4);
      const u32 sv = f2u(sum) + 0x80u;
      __hip_atomic_store((GAS u64*)(wsg + WS_QG) + ((kvh * 8 + (o >> 6)) * 4 + sp) * 512 + 64 * b + (o & 63), (T40 << 24) | (u64)(sv >> 8), RLX);
    }
    STAMP(1);
    DRAIN();
    if (QSLEEP) __builtin_amdgcn_s_sleep(QSLEEP);
    STAMP(10);
    {
      GAS u64* qgp = (GAS u64*)(wsg + WS_QG) + (kvh * 8 + b) * 4 * 512 + tid;
      u64 qv[8]; int spins = 0;
      for (;;) {
        bool ok = true;
#pragma unroll
        for (int i = 0; i < 8; ++i) qv[i] = __hip_atomic_load(qgp + (i & 3) * 512 + (i >> 2) * 256, RLX);
#pragma unroll
        for (int i = 0; i < 8; ++i) ok &= (qv[i] >> 24) == T40;
        if (__all(ok) || ++spins > SPIN_MAX) break;
        MEMFENCE(); __builtin_amdgcn_s_sleep(2);
      }
      const float qa = (u2f((u32)qv[0] << 8) + u2f((u32)qv[1] << 8)) + (u2f((u32)qv[2] << 8) + u2f((u32)qv[3] << 8));
      const float qb = (u2f((u32)qv[4] << 8) + u2f((u32)qv[5] << 8)) + (u2f((u32)qv[6] << 8) + u2f((u32)qv[7] << 8));
      *(LAS u16*)(L + L_Q + tid * 2) = (u16)cvtpk(qa, qa);
      *(LAS u16*)(L + L_Q + 512 + tid * 2) = (u16)cvtpk(qb, qb);
    }
    STAMP(15);
    wg_barrier();
    SB();
    if (role == 1 || !LATEW) { u32x4 wst[16]; LOAD_WEIGHTS(wst); STAGE_WEIGHTS(wst); }
    wg_barrier();
  } else {
    const int sw = w - 4;
    u32x4 Kb[3][4], Vb[3][4];
    const int koff = (4 * (c16 & 3) + (c16 >> 2)) * 256 + g * 16, voff = lane * 16;
#define ISSUE_K(slot, it) do { const int so_ = ((it) * 4 + sw) * 4096; \
    _Pragma("unroll") for (int u = 0; u < 4; ++u) Kb[slot][u] = LDB128(rk, koff + u * 64, so_, AUX_NT); SB(); } while (0)
#define ISSUE_V(slot, it) do { const int so_ = ((it) * 4 + sw) * 4096; \
    _Pragma("unroll") for (int u = 0; u < 4; ++u) Vb[slot][u] = LDB128(rv, voff + u * 1024, so_, AUX_NT); SB(); } while (0)
#define ISSUE(slot, it) do { ISSUE_K(slot, it); ISSUE_V(slot, it); } while (0)
    RAWBAR();
    if (SLEEP0) __builtin_amdgcn_s_sleep(SLEEP0);
    SB();
    ISSUE(0, 0);
    RAWBAR(); SB();
    ISSUE_V(1, 1);
    RAWBAR(); SB();
    ISSUE_K(1, 1);
    asm volatile("s_waitcnt vmcnt(4)" ::: "memory"); SB();
    ISSUE_K(2, 2);
    asm volatile("s_waitcnt vmcnt(4)" ::: "memory"); SB();
    ISSUE_V(2, 2);
    asm volatile("s_waitcnt lgkmcnt(0)" : "+s"(E));
    T64 = E + 1;
    wg_barrier();
    STAMP(2);
    u32x4 Qf[4];
#pragma unroll
    for (int u = 0; u < 4; ++u) Qf[u] = *(LAS u32x4*)(L + L_Q + ((lane & 3) * 128 + 32 * u + 8 * g) * 2);
    const int jx = c16 >> 2;
    const u32 m0k = jx == 0 ? 0x0000FFFFu : jx == 1 ? 0xFFFF0000u : 0u;
    const u32 m1k = jx == 2 ? 0x0000FFFFu : jx == 3 ? 0xFFFF0000u : 0u;
    float mrun = -INFINITY, lrun = 0.f;
    f32x4 O1 = {0.f, 0.f, 0.f, 0.f}, O2 = {0.f, 0.f, 0.f, 0.f};
#define PROCESS(slot) do { \
    f32x4 s_ = {0.f, 0.f, 0.f, 0.f}; \
    _Pragma("unroll") for (int u = 0; u < 4; ++u) s_ = mfma32(Kb[slot][u], Qf[u], s_); \
    const float tm_ = xrow_max(fmaxf(fmaxf(s_[0], s_[1]), fmaxf(s_[2], s_[3]))); \
    if (__any(tm_ > mrun)) { \
      const float mn_ = fmaxf(mrun, tm_), al_ = __builtin_amdgcn_exp2f((mrun - mn_) * SC_LOG2); \
      lrun *= al_; mrun = mn_; \
      const float a0_ = dpp<0x00>(al_), a1_ = dpp<0x55>(al_), a2_ = dpp<0xAA>(al_), a3_ = dpp<0xFF>(al_); \
      O1[0] *= a0_; O1[1] *= a1_; O1[2] *= a2_; O1[3] *= a3_; O2[0] *= a0_; O2[1] *= a1_; O2[2] *= a2_; O2[3] *= a3_; \
    } \
    const float mc_ = mrun * SC_LOG2; \
    _Pragma("unroll") for (int i = 0; i < 4; ++i) { \
      const float p_ = __builtin_amdgcn_exp2f(fmaf(s_[i], SC_LOG2, -mc_)); lrun += p_; \
      const u32 pv_ = cvtpk(p_, p_); const u32x2 a_ = {pv_ & m0k, pv_ & m1k}; \
      O1 = mfma16(a_, u32x2{Vb[slot][i].x, Vb[slot][i].y}, O1); O2 = mfma16(a_, u32x2{Vb[slot][i].z, Vb[slot][i].w}, O2); \
    } SB(); } while (0)
#pragma unroll 1
    for (int it = 0; it < 27; it += 3) { PROCESS(0); ISSUE(0, it + 3); PROCESS(1); ISSUE(1, it + 4); PROCESS(2); ISSUE(2, it + 5); }
    PROCESS(0); ISSUE(0, 30); PROCESS(1); ISSUE(1, 31); PROCESS(2); PROCESS(0); PROCESS(1);
    STAMP(3);
    lrun = xrow_sum(lrun);
#pragma unroll
    for (int h = 0; h < 4; ++h) *(LAS f32x2*)(L + L_MRG + ((sw * 4 + h) * 64 + lane) * 8) = f32x2{O1[h], O2[h]};
    if (lane < 4) { *(LAS float*)(L + L_ST + (lane * 4 + sw) * 4) = mrun; *(LAS float*)(L + L_ST + 64 + (lane * 4 + sw) * 4) = lrun; }
    wg_barrier();
  }
  const u32 T32 = (u32)T64;

  if (w == 0) {
    const int h = g;
    const f32x4 ma = *(LAS f32x4*)(L + L_ST + h * 16), la = *(LAS f32x4*)(L + L_ST + 64 + h * 16);
    const float M = fmaxf(fmaxf(ma[0], ma[1]), fmaxf(ma[2], ma[3]));
    float Ls = 0.f;
    f32x4 oa = {0.f, 0.f, 0.f, 0.f}, ob = {0.f, 0.f, 0.f, 0.f};
#pragma unroll
    for (int ww = 0; ww < 4; ++ww) {
      const float f = __builtin_amdgcn_exp2f((ma[ww] - M) * SC_LOG2);
      Ls = fmaf(f, la[ww], Ls);
      oa += f * *(LAS f32x4*)(L + L_MRG + ((ww * 4 + h) * 64 + 4 * c16) * 8);
      ob += f * *(LAS f32x4*)(L + L_MRG + ((ww * 4 + h) * 64 + 4 * c16) * 8 + 16);
    }
    const float inv = __builtin_amdgcn_rcpf(Ls);
    const u32x4 pk = {cvtpk(oa[0] * inv, oa[1] * inv), cvtpk(oa[2] * inv, oa[3] * inv), cvtpk(ob[0] * inv, ob[1] * inv), cvtpk(ob[2] * inv, ob[3] * inv)};
    __builtin_amdgcn_raw_buffer_store_b128(pk, rws, WS_PART + (kvh * 32 + j) * 1024 + lane * 16, 0, AUX_SC1);
    const float lam = fmaf(M, SC_LOG2, __builtin_amdgcn_logf(Ls));
    DRAIN();
    if (c16 == 0) __hip_atomic_store((GAS u64*)(wsg + WS_H1F + (kvh * 32 + j) * FS) + h, ((u64)T32 << 32) | f2u(lam), RLX);
  }
  STAMP(4);
  if (LATEW && role >= 2) {
    SB(); RAWBAR(); SB();
    if (w < 4) { u32x4 wst[16]; LOAD_WEIGHTS(wst); STAGE_WEIGHTS(wst); }
    wg_barrier();
  }

  const int rb = lane >> 3, cp = lane & 7;
  if (role == 1) {
    const int h = w >> 1, np = w & 1;
    u32x4 Bf[2][4];
    {
      const int q = (lane >> 2) & 3;
#pragma unroll
      for (int n = 0; n < 2; ++n)
#pragma unroll
        for (int u = 0; u < 4; ++u) {
          const int d0 = h * 128 + 32 * g + 8 * (q >> 1) + 4 * (q & 1) + u, d1 = d0 + 16;
          const int sg = (q >> 1) | ((g & 1) << 1);
          const u32x2 lo = tr_read(L + L_ROLE + (d0 ^ (q & 1)) * 128 + 32 * ((2 * np + n) ^ sg) + (lane & 3) * 8);
          const u32x2 hi = tr_read(L + L_ROLE + (d1 ^ (q & 1)) * 128 + 32 * ((2 * np + n) ^ sg) + (lane & 3) * 8);
          Bf[n][u] = u32x4{lo.x, lo.y, hi.x, hi.y};
        }
    }
    GAS u64* fp = (GAS u64*)(wsg + WS_H1F + (kvh * 32 + 4 * w + ((lane >> 2) & 3)) * FS) + (lane & 3);
    u64 f0; int spins = 0;
    for (;;) {
      f0 = __hip_atomic_load(fp, RLX);
      if (__all((u32)(f0 >> 32) == T32) || ++spins > SPIN_MAX) break;
      MEMFENCE(); __builtin_amdgcn_s_sleep(2);
    }
    MEMFENCE();
    u32x4 pv[4];
#pragma unroll
    for (int i = 0; i < 4; ++i) pv[i] = LDB128(rws, WS_PART + (kvh * 32 + 4 * w + i) * 1024 + lane * 16, 0, AUX_SC1);
    if (lane < 16) *(LAS u32*)(L + L_LAM + ((lane & 3) * 32 + 4 * w + (lane >> 2)) * 4) = (u32)f0;
#pragma unroll
    for (int i = 0; i < 4; ++i) *(LAS u32x4*)(L + L_P + (4 * w + i) * P_STR + lane * 16) = pv[i];
    wg_barrier();
#pragma unroll
    for (int T = 0; T < 2; ++T) {
      const f32x4 lm4 = *(LAS f32x4*)(L + L_LAM + (h * 32 + 16 * T + 4 * g) * 4);
      f32x4 a0 = {0.f, 0.f, 0.f, 0.f}, a1 = {0.f, 0.f, 0.f, 0.f};
#pragma unroll
      for (int u = 0; u < 4; ++u) {
        const u32x4 av = *(LAS u32x4*)(L + L_P + (16 * T + c16) * P_STR + h * 256 + u * 64 + g * 16);
        a0 = mfma32(av, Bf[0][u], a0); a1 = mfma32(av, Bf[1][u], a1);
      }
      const float lm = fmaxf(fmaxf(lm4[0], lm4[1]), fmaxf(lm4[2], lm4[3]));
      float e0 = __builtin_amdgcn_exp2f(lm4[0] - lm), e1 = __builtin_amdgcn_exp2f(lm4[1] - lm), e2 = __builtin_amdgcn_exp2f(lm4[2] - lm), e3 = __builtin_amdgcn_exp2f(lm4[3] - lm);
      const float inv = __builtin_amdgcn_rcpf((e0 + e1) + (e2 + e3));
      e0 *= inv; e1 *= inv; e2 *= inv; e3 *= inv;
      *(LAS float*)(L + L_RED + ((h * 8 + 4 * T + g) * 64 + 32 * np + c16) * 4) = (e0 * a0[0] + e1 * a0[1]) + (e2 * a0[2] + e3 * a0[3]);
      *(LAS float*)(L + L_RED + ((h * 8 + 4 * T + g) * 64 + 32 * np + 16 + c16) * 4) = (e0 * a1[0] + e1 * a1[1]) + (e2 * a1[2] + e3 * a1[3]);
    }
    wg_barrier();
    STAMP(5);
    if (w == 0) {
      f32x4 sa = {0.f, 0.f, 0.f, 0.f}, sb = {0.f, 0.f, 0.f, 0.f};
#pragma unroll
      for (int hh = 0; hh < 4; ++hh) { sa += *(LAS f32x4*)(L + L_RED + ((hh * 8 + rb) * 64 + 8 * cp) * 4); sb += *(LAS f32x4*)(L + L_RED + ((hh * 8 + rb) * 64 + 8 * cp) * 4 + 16); }
      const u32x4 pk = {cvtpk(sa[0], sa[1]), cvtpk(sa[2], sa[3]), cvtpk(sb[0], sb[1]), cvtpk(sb[2], sb[3])};
      __builtin_amdgcn_raw_buffer_store_b128(pk, rws, WS_Y + ((kvh * 8 + rb) * DM + 64 * b + 8 * cp) * 2, 0, AUX_SC1);
      DRAIN();
      if (lane == 0) __hip_atomic_store((GAS u64*)(wsg + WS_H2F + (kvh * 8 + b) * FS), T64, RLX);
    }
    STAMP(6);
  } else if (role == 2) {
    const int mat = w & 1, kq = w >> 1;
    u32x4 Bf[2][4];
    {
      const int q = (lane >> 2) & 3;
#pragma unroll
      for (int n = 0; n < 2; ++n)
#pragma unroll
        for (int u = 0; u < 4; ++u) {
          LAS unsigned char* p = L + L_ROLE + mat * 32768 + (128 * kq + 32 * u + 8 * g + q) * 64 + 32 * (n ^ (g & 1)) + (lane & 3) * 8;
          const u32x2 lo = tr_read(p), hi = tr_read(p + 256);
          Bf[n][u] = u32x4{lo.x, lo.y, hi.x, hi.y};
        }
    }
    const u32x4 xv = LDB128(rx, (w * DM + 8 * lane) * 2, 0, 0);
    if (w == 0) {
      GAS u64* fp = (GAS u64*)(wsg + WS_H2F + lane * FS); int spins = 0;
      for (;;) {
        const u64 f0 = __hip_atomic_load(fp, RLX);
        if (__all(f0 == T64) || ++spins > SPIN_MAX) break;
        MEMFENCE(); __builtin_amdgcn_s_sleep(1);
      }
    }
    wg_barrier();
    STAMP(7);
    u32x4 yv[8];
#pragma unroll
    for (int i = 0; i < 8; ++i) yv[i] = LDB128(rws, WS_Y + ((i * 8 + w) * DM + 8 * lane) * 2, 0, AUX_SC1);
    float v[8];
#pragma unroll
    for (int i = 0; i < 4; ++i) { v[2 * i] = bflo(xv[i]); v[2 * i + 1] = bfhi(xv[i]); }
#pragma unroll
    for (int i = 0; i < 8; ++i) {
#pragma unroll
      for (int e = 0; e < 4; ++e) { v[2 * e] += bflo(yv[i][e]); v[2 * e + 1] += bfhi(yv[i][e]); }
    }
    *(LAS u32x4*)(L + L_A + w * A_STR + lane * 16) = ln_row(v);
    wg_barrier();
    STAMP(8);
    f32x4 a0 = {0.f, 0.f, 0.f, 0.f}, a1 = {0.f, 0.f, 0.f, 0.f};
#pragma unroll
    for (int u = 0; u < 4; ++u) {
      const u32x4 av = *(LAS u32x4*)(L + L_A + (lane & 7) * A_STR + (128 * kq + 32 * u + 8 * g) * 2);
      a0 = mfma32(av, Bf[0][u], a0); a1 = mfma32(av, Bf[1][u], a1);
    }
    if (lane < 32) {
#pragma unroll
      for (int r = 0; r < 4; ++r) {
        *(LAS float*)(L + L_RED + (((kq * 2 + mat) * 8 + 4 * g + r) * 32 + c16) * 4) = a0[r];
        *(LAS float*)(L + L_RED + (((kq * 2 + mat) * 8 + 4 * g + r) * 32 + 16 + c16) * 4) = a1[r];
      }
    }
    wg_barrier();
    if (w == 0) {
      f32x4 ga = {0.f, 0.f, 0.f, 0.f}, up = {0.f, 0.f, 0.f, 0.f};
#pragma unroll
      for (int q = 0; q < 4; ++q) { ga += *(LAS f32x4*)(L + L_RED + (((q * 2 + 0) * 8 + rb) * 32 + 4 * cp) * 4); up += *(LAS f32x4*)(L + L_RED + (((q * 2 + 1) * 8 + rb) * 32 + 4 * cp) * 4); }
      float r[4];
#pragma unroll
      for (int e = 0; e < 4; ++e) {
        const float z = ga[e];
        const float t = z * fmaf(z * z, 0.044715f * 0.7978845608028654f, 0.7978845608028654f);
        const float sg = __builtin_amdgcn_rcpf(1.0f + __builtin_amdgcn_exp2f(t * (-2.0f * 1.4426950408889634f)));
        r[e] = z * sg * up[e];
      }
      __builtin_amdgcn_raw_buffer_store_b64(u32x2{cvtpk(r[0], r[1]), cvtpk(r[2], r[3])}, rws, WS_G + (rb * FF + 32 * cgu + 4 * cp) * 2, 0, AUX_SC1);
      DRAIN();
      if (lane == 0) __hip_atomic_store((GAS u64*)(wsg + WS_H3F + cgu * FS), T64, RLX);
    }
    STAMP(9);
  } else if (role == 3) {
    u32x4 Bf[7];
#pragma unroll
    for (int u = 0; u < 7; ++u) Bf[u] = bfrag(L + L_ROLE + (224 * w + 32 * u) * 32 + trl, 128);
    f32x2 hres = {0.f, 0.f};
    if (w == 0) {
      const u32 xw = *(const GAS u32*)((const GAS unsigned char*)x + (rb * DM + 16 * cdn + 2 * cp) * 2);
      GAS u64* fp = (GAS u64*)(wsg + WS_H2F + ((lane & 7) * 8 + (cdn >> 2)) * FS); int spins = 0;
      for (;;) { const u64 f0 = __hip_atomic_load(fp, RLX); if (__all(f0 == T64) || ++spins > SPIN_MAX) break; MEMFENCE(); __builtin_amdgcn_s_sleep(2); }
      MEMFENCE();
      u32 yy[8];
#pragma unroll
      for (int q = 0; q < 8; ++q) yy[q] = __builtin_amdgcn_raw_buffer_load_b32(rws, WS_Y + ((q * 8 + rb) * DM + 16 * cdn + 2 * cp) * 2, 0, AUX_SC1);
      hres = f32x2{bflo(xw), bfhi(xw)};
#pragma unroll
      for (int q = 0; q < 8; ++q) hres += f32x2{bflo(yy[q]), bfhi(yy[q])};
      GAS u64* gp = (GAS u64*)(wsg + WS_H3F + lane * FS); spins = 0;
      for (;;) {
        const u64 f0 = lane < NGU ? __hip_atomic_load(gp, RLX) : T64;
        if (__all(f0 == T64) || ++spins > SPIN_MAX) break;
        MEMFENCE(); __builtin_amdgcn_s_sleep(1);
      }
      if (cdn == 0 && lane == 0) __hip_atomic_store((GAS u64*)(wsg + WS_E), T64, RLX);
    }
    wg_barrier();
    STAMP(9);
    {
      u32x4 gv[4];
#pragma unroll
      for (int i = 0; i < 4; ++i) gv[i] = LDB128(rws, (i == 3 && w >= 4) ? WS_END : WS_G + ((w + 8 * i) * 64 + lane) * 16, 0, AUX_SC1);
#pragma unroll
      for (int i = 0; i < 4; ++i) { const int ci = (w + 8 * i) * 64 + lane, row = ci / 224; if (i < 3 || w < 4) *(LAS u32x4*)(L + L_P + row * G_STR + (ci - row * 224) * 16) = gv[i]; }
    }
    wg_barrier();
    f32x4 acc = {0.f, 0.f, 0.f, 0.f};
#pragma unroll
    for (int u = 0; u < 7; ++u) acc = mfma32(*(LAS u32x4*)(L + L_P + (lane & 7) * G_STR + ((7 * w + u) * 32 + 8 * g) * 2), Bf[u], acc);
    if (lane < 32) {
#pragma unroll
      for (int r = 0; r < 4; ++r) *(LAS float*)(L + L_RED + ((w * 8 + 4 * g + r) * 16 + c16) * 4) = acc[r];
    }
    wg_barrier();
    if (w == 0) {
      f32x2 s = hres;
#pragma unroll
      for (int ww = 0; ww < 8; ++ww) s += *(LAS f32x2*)(L + L_RED + ((ww * 8 + rb) * 16 + 2 * cp) * 4);
      *(GAS u32*)((GAS unsigned char*)out + (rb * DM + 16 * cdn + 2 * cp) * 2) = cvtpk(s[0], s[1]);
    }
    STAMP(8);
  }
  STAMP_FLUSH();
}

extern "C" void kernel_launch(void* const* d_in, const int* in_sizes, int n_in, void* d_out, int out_size, void* d_ws, size_t ws_size, hipStream_t stream) {
  fused_decode_block<<<NWG, NTHR, 0, stream>>>((const u16*)d_in[0], (const u16*)d_in[1], (const u16*)d_in[2], (const u16*)d_in[3], (const u16*)d_in[4],
                                              (const u16*)d_in[6], (const u16*)d_in[7], (const u16*)d_in[8], (u16*)d_out, (unsigned char*)d_ws);
}
```

```cpp
#include <hip/hip_runtime.h>
#include <hip/hip_bf16.h>
#include <stdint.h>

typedef unsigned short u16;
typedef unsigned u32;
typedef unsigned long long u64;
typedef u32 u32x4 __attribute__((ext_vector_type(4)));
typedef u32 u32x2 __attribute__((ext_vector_type(2)));
typedef float f32x4 __attribute__((ext_vector_type(4)));
typedef float f32x2 __attribute__((ext_vector_type(2)));
typedef short s16x8 __attribute__((ext_vector_type(8)));
typedef short s16x4 __attribute__((ext_vector_type(4)));
typedef __bf16 bf16x8_t __attribute__((ext_vector_type(8)));
#define GAS __attribute__((address_space(1)))
#define LAS __attribute__((address_space(3)))
#define RLX __ATOMIC_RELAXED, __HIP_MEMORY_SCOPE_AGENT

#define QPOS 0
#define HOLD 1
#define PASSES 1
#define SLEEP0 8
#define QSLEEP 4
#define LATEW 1
#define VB 2
#define WAUX 0
#define STAMPS 0

constexpr int NB = 8, DM = 512, HQ = 32, HKV = 8, SK = 8192, DH = 128, FF = 1792;
constexpr int NWG = 256, NTHR = 512;
constexpr int NGU = FF / 32;
constexpr int TOK_WG = SK / 4;
constexpr int NIT = TOK_WG / 16 / 8;
constexpr float SC_LOG2 = 0.08838834764831845f * 1.4426950408889634f;
constexpr float LN_EPS = 1e-5f;
constexpr int AUX_NT = 2, AUX_SC1 = 16;
constexpr int SPIN_MAX = 1 << 16;

constexpr int FS = 256;
constexpr int WS_E    = 0;
constexpr int WS_H1F  = 4096;
constexpr int WS_H2F  = WS_H1F + 256 * FS;
constexpr int WS_H3F  = WS_H2F + 64 * FS;
constexpr int WS_QG   = WS_H3F + 64 * FS;
constexpr int WS_PART = WS_QG + HKV * NB * 4 * 512 * 8;
constexpr int WS_Y    = WS_PART + HKV * 32 * 4 * 128 * 2;
constexpr int WS_G    = WS_Y + HKV * NB * DM * 2;
constexpr int WS_END  = WS_G + NB * FF * 2;
constexpr int WS_STAMP = 2 << 20;

constexpr int L_ROLE = 0;
constexpr int L_WQ = 0;
constexpr int L_MRG = 65536;
constexpr int A_STR = 1040;
constexpr int L_A = L_MRG + 16384;
constexpr int L_RED = L_A + 8 * A_STR;
constexpr int L_Q = L_RED + 8192;
constexpr int L_ST = L_Q + 1024;
constexpr int L_LAM = L_ST + 256;
constexpr int P_STR = 1040;
constexpr int L_P = L_LAM + 512;
constexpr int G_STR = 3600;
constexpr int LDS_BYTES = L_P + 32 * P_STR;

typedef __bf16 bf16x2_t __attribute__((ext_vector_type(2)));
__device__ __forceinline__ u32 cvtpk(float lo, float hi) { const bf16x2_t r = {(__bf16)lo, (__bf16)hi}; return __builtin_bit_cast(u32, r); }
__device__ __forceinline__ float u2f(u32 v) { return __builtin_bit_cast(float, v); }
__device__ __forceinline__ u32 f2u(float v) { return __builtin_bit_cast(u32, v); }
__device__ __forceinline__ float bflo(u32 v) { return __builtin_bit_cast(float, v << 16); }
__device__ __forceinline__ float bfhi(u32 v) { return __builtin_bit_cast(float, v & 0xffff0000u); }
template <int CTRL> __device__ __forceinline__ float dpp(float x) { return __builtin_bit_cast(float, __builtin_amdgcn_mov_dpp(__builtin_bit_cast(int, x), CTRL, 0xf, 0xf, true)); }
__device__ __forceinline__ float xrow_max(float x) {
  auto s = __builtin_amdgcn_permlane16_swap(f2u(x), f2u(x), false, false);
  x = fmaxf(u2f(s[0]), u2f(s[1]));
  auto t = __builtin_amdgcn_permlane32_swap(f2u(x), f2u(x), false, false);
  return fmaxf(u2f(t[0]), u2f(t[1]));
}
__device__ __forceinline__ float xrow_sum(float x) {
  auto s = __builtin_amdgcn_permlane16_swap(f2u(x), f2u(x), false, false);
  x = u2f(s[0]) + u2f(s[1]);
  auto t = __builtin_amdgcn_permlane32_swap(f2u(x), f2u(x), false, false);
  return u2f(t[0]) + u2f(t[1]);
}
__device__ __forceinline__ float wave_sum(float x) {
  x += dpp<0xB1>(x); x += dpp<0x4E>(x); x += dpp<0x141>(x); x += dpp<0x140>(x);
  return xrow_sum(x);
}
__device__ __forceinline__ f32x4 mfma32(u32x4 a, u32x4 b, f32x4 c) {
  return __builtin_amdgcn_mfma_f32_16x16x32_bf16(__builtin_bit_cast(bf16x8_t, a), __builtin_bit_cast(bf16x8_t, b), c, 0, 0, 0);
}
__device__ __forceinline__ f32x4 mfma16(u32x2 a, u32x2 b, f32x4 c) {
  return __builtin_amdgcn_mfma_f32_16x16x16bf16_1k(__builtin_bit_cast(s16x4, a), __builtin_bit_cast(s16x4, b), c, 0, 0, 0);
}
typedef short v4i16_t __attribute__((ext_vector_type(4)));
__device__ __forceinline__ u32x2 tr_read(LAS unsigned char* p) {
  return __builtin_bit_cast(u32x2, __builtin_amdgcn_ds_read_tr16_b64_v4i16((LAS v4i16_t*)p));
}
__device__ __forceinline__ u32x4 bfrag(LAS unsigned char* p, int second_off) {
  const u32x2 a = tr_read(p), b = tr_read(p + second_off);
  return u32x4{a.x, a.y, b.x, b.y};
}
__device__ __forceinline__ __amdgpu_buffer_rsrc_t mkrs(const void* p, u32 bytes) { return __builtin_amdgcn_make_buffer_rsrc((void*)p, 0, bytes, 0x00020000); }
template <int V> struct ic { static constexpr int value = V; };
#define LDB128(rs, vo, so, aux) __builtin_amdgcn_raw_buffer_load_b128(rs, vo, so, aux)
#define SB() __builtin_amdgcn_sched_barrier(0)
#define DRAIN() asm volatile("s_waitcnt vmcnt(0)" ::: "memory")
#define MEMFENCE() asm volatile("" ::: "memory")
#define RAWBAR() asm volatile("s_barrier" ::: "memory")
__device__ __forceinline__ void wg_barrier() { asm volatile("s_waitcnt lgkmcnt(0)\n\ts_barrier" ::: "memory"); }

__device__ __forceinline__ u32x4 ln_row(const float (&v)[8]) {
  float s = ((v[0] + v[1]) + (v[2] + v[3])) + ((v[4] + v[5]) + (v[6] + v[7]));
  const float mean = wave_sum(s) * (1.0f / DM);
  float d[8], q = 0.f;
#pragma unroll
  for (int i = 0; i < 8; ++i) { d[i] = v[i] - mean; q = fmaf(d[i], d[i], q); }
  const float var = wave_sum(q) * (1.0f / DM);
  const float rs = __builtin_amdgcn_rsqf(var + LN_EPS);
  return u32x4{cvtpk(d[0] * rs, d[1] * rs), cvtpk(d[2] * rs, d[3] * rs), cvtpk(d[4] * rs, d[5] * rs), cvtpk(d[6] * rs, d[7] * rs)};
}

#define STAMP(i) do {} while (0)
#define STAMP_FLUSH() do {} while (0)

__global__ void __launch_bounds__(NTHR, 2)
fused_decode_block(const u16* __restrict__ x, const u16* __restrict__ Kc, const u16* __restrict__ Vc, const u16* __restrict__ wq, const u16* __restrict__ wo,
                   const u16* __restrict__ wg, const u16* __restrict__ wu, const u16* __restrict__ wd, u16* __restrict__ out, unsigned char* __restrict__ ws) {
  __shared__ __attribute__((aligned(16))) unsigned char lds_raw[LDS_BYTES];
  LAS unsigned char* L = (LAS unsigned char*)lds_raw;
  const int tid = threadIdx.x, lane = tid & 63, w = __builtin_amdgcn_readfirstlane(tid >> 6);
  const int blk = blockIdx.x, kvh = blk & 7, j = blk >> 3, b = j >> 2, sp = j & 3;
  const int g = lane >> 4, c16 = lane & 15;
  GAS unsigned char* wsg = (GAS unsigned char*)ws;
  STAMP(0);

  u64 E;
  asm volatile("s_load_dwordx2 %0, %1, 0x0" : "=s"(E) : "s"(ws));
  const __amdgpu_buffer_rsrc_t rx = mkrs(x, NB * DM * 2);
  const __amdgpu_buffer_rsrc_t rwq = mkrs(wq, DM * HQ * DH * 2);
  const size_t kvbase = ((size_t)(b * HKV + kvh) * SK + (size_t)sp * TOK_WG) * DH;
  const __amdgpu_buffer_rsrc_t rk = mkrs(Kc + kvbase, TOK_WG * DH * 2);
  const __amdgpu_buffer_rsrc_t rv = mkrs(Vc + kvbase, TOK_WG * DH * 2);
  const __amdgpu_buffer_rsrc_t rws = mkrs(ws, WS_END);

  const int trl = (8 * g + ((lane >> 2) & 3)) * 32 + (lane & 3) * 8;
  const int jj = 3 * b + sp - 1;
  const int role = sp == 0 ? 1 : (jj < 7 ? 2 : (jj < 11 ? 3 : 0));
  const int cgu = kvh * 7 + jj, cdn = kvh * 4 + (jj - 7);
#define LOAD_WEIGHTS(wst) do { \
      const u16* p0 = role == 1 ? wo : (role == 2 ? wg : wd); \
      const u16* p1 = role == 2 ? wu : p0; \
      const u32 n0b = role == 0 ? 0u : (role == 1 ? (u32)(HQ * DH * DM * 2) : (u32)(DM * FF * 2)); \
      const u32 n1b = role == 3 ? 0u : n0b; \
      const __amdgpu_buffer_rsrc_t r0 = mkrs(p0, n0b), r1 = mkrs(p1, n0b), r2 = mkrs(p1, n1b); \
      const int v0o = role == 1 ? ((kvh * 512 + 128 * w + (lane >> 3)) * DM + 64 * b + 8 * (lane & 7)) * 2 \
                    : role == 2 ? ((128 * w + (lane >> 2)) * FF + 32 * cgu + 8 * (lane & 3)) * 2 \
                                : ((448 * w + (lane >> 1)) * DM + 16 * cdn + 8 * (lane & 1)) * 2; \
      const int st = role == 1 ? 8 * DM * 2 : role == 2 ? 16 * FF * 2 : 32 * DM * 2; \
      const int v8o = role == 2 ? v0o : v0o + 8 * st; \
      _Pragma("unroll") for (int i = 0; i < 8; ++i) wst[i] = LDB128(r0, v0o + i * st, 0, WAUX); \
      _Pragma("unroll") for (int i = 0; i < 6; ++i) wst[8 + i] = LDB128(r1, v8o + i * st, 0, WAUX); \
      _Pragma("unroll") for (int i = 6; i < 8; ++i) wst[8 + i] = LDB128(r2, v8o + i * st, 0, WAUX); \
      SB(); } while (0)
#define STAGE_WEIGHTS(wst) do { \
    if (role == 1) { \
      _Pragma("unroll") for (int i = 0; i < 16; ++i) { const int r = 128 * w + 8 * i + (lane >> 3), ch = lane & 7; \
        *(LAS u32x4*)(L + L_ROLE + (r ^ ((r >> 2) & 1)) * 128 + 32 * ((ch >> 1) ^ (((r >> 3) & 1) | (((r >> 5) & 1) << 1))) + 16 * (ch & 1)) = wst[i]; } \
    } else if (role == 2) { \
      _Pragma("unroll") for (int i = 0; i < 16; ++i) { const int r = 128 * w + 16 * (i & 7) + (lane >> 2), ch = lane & 3; \
        *(LAS u32x4*)(L + L_ROLE + (i >> 3) * 32768 + r * 64 + 32 * ((ch >> 1) ^ ((r >> 3) & 1)) + 16 * (ch & 1)) = wst[i]; } \
    } else if (role == 3) { \
      _Pragma("unroll") for (int i = 0; i < 14; ++i) *(LAS u32x4*)(L + L_ROLE + (448 * w + 32 * i) * 32 + lane * 16) = wst[i]; \
    } } while (0)
  u64 T64;
  if (w < 4) {
    const u32x4 xr0 = LDB128(rx, (2 * w * DM + 8 * lane) * 2, 0, 0), xr1 = LDB128(rx, ((2 * w + 1) * DM + 8 * lane) * 2, 0, 0);
    u32x4 wqr[4];
#pragma unroll
    for (int i = 0; i < 4; ++i) wqr[i] = LDB128(rwq, ((128 * sp + 32 * w + 8 * i + (lane >> 3)) * (HQ * DH) + kvh * 512 + 64 * b + 8 * (lane & 7)) * 2, 0, WAUX);
    SB(); RAWBAR(); SB();
    float v0[8], v1[8];
#pragma unroll
    for (int i = 0; i < 4; ++i) { v0[2 * i] = bflo(xr0[i]); v0[2 * i + 1] = bfhi(xr0[i]); v1[2 * i] = bflo(xr1[i]); v1[2 * i + 1] = bfhi(xr1[i]); }
    const u32x4 n0 = ln_row(v0), n1 = ln_row(v1);
    *(LAS u32x4*)(L + L_A + 2 * w * A_STR + lane * 16) = n0;
    *(LAS u32x4*)(L + L_A + (2 * w + 1) * A_STR + lane * 16) = n1;
#pragma unroll
    for (int i = 0; i < 4; ++i) *(LAS u32x4*)(L + L_WQ + (32 * w + 8 * i + (lane >> 3)) * 144 + (lane & 7) * 16) = wqr[i];
    STAMP(12);
    wg_barrier();
    STAMP(13);
    const u32x4 a = *(LAS u32x4*)(L + L_A + (lane & 7) * A_STR + (128 * sp + 32 * w + 8 * g) * 2);
#pragma unroll
    for (int nt = 0; nt < 4; ++nt) {
      LAS unsigned char* p = L + L_WQ + (32 * w + 8 * g + ((lane >> 2) & 3)) * 144 + nt * 32 + (lane & 3) * 8;
      const u32x2 lo = tr_read(p), hi = tr_read(p + 4 * 144);
      const f32x4 acc = mfma32(a, u32x4{lo.x, lo.y, hi.x, hi.y}, f32x4{0.f, 0.f, 0.f, 0.f});
      if (lane < 32) {
#pragma unroll
        for (int r = 0; r < 4; ++r) *(LAS float*)(L + L_P + ((w * 8 + 4 * g + r) * 64 + 16 * nt + c16) * 4) = acc[r];
      }
    }
    wg_barrier();
    STAMP(14);
    asm volatile("s_waitcnt lgkmcnt(0)" : "+s"(E));
    T64 = E + 1;
    const u64 T40 = T64 & 0xFFFFFFFFFFull;
#pragma unroll
    for (int i = 0; i < 2; ++i) {
      const int o = tid + 256 * i;
      float sum = 0.f;
#pragma unroll
      for (int ww = 0; ww < 4; ++ww) sum += *(LAS float*)(L + L_P + (ww * 512 + o) * 4);
      const u32 sv = f2u(sum) + 0x80u;
      __hip_atomic_store((GAS u64*)(wsg + WS_QG) + ((kvh * 8 + (o >> 6)) * 4 + sp) * 512 + 64 * b + (o & 63), (T40 << 24) | (u64)(sv >> 8), RLX);
    }
    STAMP(1);
    DRAIN();
    if (QSLEEP) __builtin_amdgcn_s_sleep(QSLEEP);
    STAMP(10);
    {
      GAS u64* qgp = (GAS u64*)(wsg + WS_QG) + (kvh * 8 + b) * 4 * 512 + tid;
      u64 qv[8]; int spins = 0;
      for (;;) {
        bool ok = true;
#pragma unroll
        for (int i = 0; i < 8; ++i) qv[i] = __hip_atomic_load(qgp + (i & 3) * 512 + (i >> 2) * 256, RLX);
#pragma unroll
        for (int i = 0; i < 8; ++i) ok &= (qv[i] >> 24) == T40;
        if (__all(ok) || ++spins > SPIN_MAX) break;
        MEMFENCE(); __builtin_amdgcn_s_sleep(2);
      }
      const float qa = (u2f((u32)qv[0] << 8) + u2f((u32)qv[1] << 8)) + (u2f((u32)qv[2] << 8) + u2f((u32)qv[3] << 8));
      const float qb = (u2f((u32)qv[4] << 8) + u2f((u32)qv[5] << 8)) + (u2f((u32)qv[6] << 8) + u2f((u32)qv[7] << 8));
      *(LAS u16*)(L + L_Q + tid * 2) = (u16)cvtpk(qa, qa);
      *(LAS u16*)(L + L_Q + 512 + tid * 2) = (u16)cvtpk(qb, qb);
    }
    STAMP(15);
    wg_barrier();
    SB();
    if (role == 1 || !LATEW) { u32x4 wst[16]; LOAD_WEIGHTS(wst); STAGE_WEIGHTS(wst); }
    wg_barrier();
  } else {
    const int sw = w - 4;
    const int koff = (4 * (c16 & 3) + (c16 >> 2)) * 256 + g * 16, voff = lane * 16;
    const bool vkeep = b >= VB;
#define ISSUE_K(slot, it) do { const int so_ = ((it) * 4 + sw) * 4096; \
    _Pragma("unroll") for (int u = 0; u < 4; ++u) Kb[slot][u] = LDB128(rk, koff + u * 64, so_, AUX_NT); SB(); } while (0)
#define ISSUE_V(slot, it) do { const int so_ = ((it) * 4 + sw) * 4096; \
    _Pragma("unroll") for (int u = 0; u < 4; ++u) Vb[slot][u] = LDB128(rv, voff + u * 1024, so_, VAUX); SB(); } while (0)
#define ISSUE(slot, it) do { ISSUE_K(slot, it); ISSUE_V(slot, it); } while (0)
    float mrun = -INFINITY, lrun = 0.f;
    f32x4 O1 = {0.f, 0.f, 0.f, 0.f}, O2 = {0.f, 0.f, 0.f, 0.f};
    auto stream = [&](auto vaux) __attribute__((always_inline)) {
    constexpr int VAUX = decltype(vaux)::value;
    u32x4 Kb[3][4], Vb[3][4];
    RAWBAR();
    if (SLEEP0) __builtin_amdgcn_s_sleep(SLEEP0);
    SB();
    ISSUE(0, 0);
    RAWBAR(); SB();
    ISSUE_V(1, 1);
    RAWBAR(); SB();
    ISSUE_K(1, 1);
    asm volatile("s_waitcnt vmcnt(4)" ::: "memory"); SB();
    ISSUE_K(2, 2);
    asm volatile("s_waitcnt vmcnt(4)" ::: "memory"); SB();
    ISSUE_V(2, 2);
    wg_barrier();
    STAMP(2);
    u32x4 Qf[4];
#pragma unroll
    for (int u = 0; u < 4; ++u) Qf[u] = *(LAS u32x4*)(L + L_Q + ((lane & 3) * 128 + 32 * u + 8 * g) * 2);
    const int jx = c16 >> 2;
    const u32 m0k = jx == 0 ? 0x0000FFFFu : jx == 1 ? 0xFFFF0000u : 0u;
    const u32 m1k = jx == 2 ? 0x0000FFFFu : jx == 3 ? 0xFFFF0000u : 0u;
#define PROCESS(slot) do { \
    f32x4 s_ = {0.f, 0.f, 0.f, 0.f}; \
    _Pragma("unroll") for (int u = 0; u < 4; ++u) s_ = mfma32(Kb[slot][u], Qf[u], s_); \
    const float tm_ = xrow_max(fmaxf(fmaxf(s_[0], s_[1]), fmaxf(s_[2], s_[3]))); \
    if (__any(tm_ > mrun)) { \
      const float mn_ = fmaxf(mrun, tm_), al_ = __builtin_amdgcn_exp2f((mrun - mn_) * SC_LOG2); \
      lrun *= al_; mrun = mn_; \
      const float a0_ = dpp<0x00>(al_), a1_ = dpp<0x55>(al_), a2_ = dpp<0xAA>(al_), a3_ = dpp<0xFF>(al_); \
      O1[0] *= a0_; O1[1] *= a1_; O1[2] *= a2_; O1[3] *= a3_; O2[0] *= a0_; O2[1] *= a1_; O2[2] *= a2_; O2[3] *= a3_; \
    } \
    const float mc_ = mrun * SC_LOG2; \
    _Pragma("unroll") for (int i = 0; i < 4; ++i) { \
      const float p_ = __builtin_amdgcn_exp2f(fmaf(s_[i], SC_LOG2, -mc_)); lrun += p_; \
      const u32 pv_ = cvtpk(p_, p_); const u32x2 a_ = {pv_ & m0k, pv_ & m1k}; \
      O1 = mfma16(a_, u32x2{Vb[slot][i].x, Vb[slot][i].y}, O1); O2 = mfma16(a_, u32x2{Vb[slot][i].z, Vb[slot][i].w}, O2); \
    } SB(); } while (0)
#pragma unroll 1
    for (int it = 0; it < 27; it += 3) { PROCESS(0); ISSUE(0, it + 3); PROCESS(1); ISSUE(1, it + 4); PROCESS(2); ISSUE(2, it + 5); }
    PROCESS(0); ISSUE(0, 30); PROCESS(1); ISSUE(1, 31); PROCESS(2); PROCESS(0); PROCESS(1);
    };
    if (vkeep) stream(ic<0>{}); else stream(ic<AUX_NT>{});
    asm volatile("s_waitcnt lgkmcnt(0)" : "+s"(E));
    T64 = E + 1;
    STAMP(3);
    lrun = xrow_sum(lrun);
#pragma unroll
    for (int h = 0; h < 4; ++h) *(LAS f32x2*)(L + L_MRG + ((sw * 4 + h) * 64 + lane) * 8) = f32x2{O1[h], O2[h]};
    if (lane < 4) { *(LAS float*)(L + L_ST + (lane * 4 + sw) * 4) = mrun; *(LAS float*)(L + L_ST + 64 + (lane * 4 + sw) * 4) = lrun; }
    wg_barrier();
  }
  const u32 T32 = (u32)T64;

  if (w == 0) {
    const int h = g;
    const f32x4 ma = *(LAS f32x4*)(L + L_ST + h * 16), la = *(LAS f32x4*)(L + L_ST + 64 + h * 16);
    const float M = fmaxf(fmaxf(ma[0], ma[1]), fmaxf(ma[2], ma[3]));
    float Ls = 0.f;
    f32x4 oa = {0.f, 0.f, 0.f, 0.f}, ob = {0.f, 0.f, 0.f, 0.f};
#pragma unroll
    for (int ww = 0; ww < 4; ++ww) {
      const float f = __builtin_amdgcn_exp2f((ma[ww] - M) * SC_LOG2);
      Ls = fmaf(f, la[ww], Ls);
      oa += f * *(LAS f32x4*)(L + L_MRG + ((ww * 4 + h) * 64 + 4 * c16) * 8);
      ob += f * *(LAS f32x4*)(L + L_MRG + ((ww * 4 + h) * 64 + 4 * c16) * 8 + 16);
    }
    const float inv = __builtin_amdgcn_rcpf(Ls);
    const u32x4 pk = {cvtpk(oa[0] * inv, oa[1] * inv), cvtpk(oa[2] * inv, oa[3] * inv), cvtpk(ob[0] * inv, ob[1] * inv), cvtpk(ob[2] * inv, ob[3] * inv)};
    __builtin_amdgcn_raw_buffer_store_b128(pk, rws, WS_PART + (kvh * 32 + j) * 1024 + lane * 16, 0, AUX_SC1);
    const float lam = fmaf(M, SC_LOG2, __builtin_amdgcn_logf(Ls));
    DRAIN();
    if (c16 == 0) __hip_atomic_store((GAS u64*)(wsg + WS_H1F + (kvh * 32 + j) * FS) + h, ((u64)T32 << 32) | f2u(lam), RLX);
  }
  STAMP(4);
  if (LATEW && role >= 2) {
    SB(); RAWBAR(); SB();
    if (w < 4) { u32x4 wst[16]; LOAD_WEIGHTS(wst); STAGE_WEIGHTS(wst); }
    wg_barrier();
  }

  const int rb = lane >> 3, cp = lane & 7;
  if (role == 1) {
    const int h = w >> 1, np = w & 1;
    u32x4 Bf[2][4];
    {
      const int q = (lane >> 2) & 3;
#pragma unroll
      for (int n = 0; n < 2; ++n)
#pragma unroll
        for (int u = 0; u < 4; ++u) {
          const int d0 = h * 128 + 32 * g + 8 * (q >> 1) + 4 * (q & 1) + u, d1 = d0 + 16;
          const int sg = (q >> 1) | ((g & 1) << 1);
          const u32x2 lo = tr_read(L + L_ROLE + (d0 ^ (q & 1)) * 128 + 32 * ((2 * np + n) ^ sg) + (lane & 3) * 8);
          const u32x2 hi = tr_read(L + L_ROLE + (d1 ^ (q & 1)) * 128 + 32 * ((2 * np + n) ^ sg) + (lane & 3) * 8);
          Bf[n][u] = u32x4{lo.x, lo.y, hi.x, hi.y};
        }
    }
    GAS u64* fp = (GAS u64*)(wsg + WS_H1F + (kvh * 32 + 4 * w + ((lane >> 2) & 3)) * FS) + (lane & 3);
    u64 f0; int spins = 0;
    for (;;) {
      f0 = __hip_atomic_load(fp, RLX);
      if (__all((u32)(f0 >> 32) == T32) || ++spins > SPIN_MAX) break;
      MEMFENCE(); __builtin_amdgcn_s_sleep(2);
    }
    MEMFENCE();
    u32x4 pv[4];
#pragma unroll
    for (int i = 0; i < 4; ++i) pv[i] = LDB128(rws, WS_PART + (kvh * 32 + 4 * w + i) * 1024 + lane * 16, 0, AUX_SC1);
    if (lane < 16) *(LAS u32*)(L + L_LAM + ((lane & 3) * 32 + 4 * w + (lane >> 2)) * 4) = (u32)f0;
#pragma unroll
    for (int i = 0; i < 4; ++i) *(LAS u32x4*)(L + L_P + (4 * w + i) * P_STR + lane * 16) = pv[i];
    wg_barrier();
#pragma unroll
    for (int T = 0; T < 2; ++T) {
      const f32x4 lm4 = *(LAS f32x4*)(L + L_LAM + (h * 32 + 16 * T + 4 * g) * 4);
      f32x4 a0 = {0.f, 0.f, 0.f, 0.f}, a1 = {0.f, 0.f, 0.f, 0.f};
#pragma unroll
      for (int u = 0; u < 4; ++u) {
        const u32x4 av = *(LAS u32x4*)(L + L_P + (16 * T + c16) * P_STR + h * 256 + u * 64 + g * 16);
        a0 = mfma32(av, Bf[0][u], a0); a1 = mfma32(av, Bf[1][u], a1);
      }
      const float lm = fmaxf(fmaxf(lm4[0], lm4[1]), fmaxf(lm4[2], lm4[3]));
      float e0 = __builtin_amdgcn_exp2f(lm4[0] - lm), e1 = __builtin_amdgcn_exp2f(lm4[1] - lm), e2 = __builtin_amdgcn_exp2f(lm4[2] - lm), e3 = __builtin_amdgcn_exp2f(lm4[3] - lm);
      const float inv = __builtin_amdgcn_rcpf((e0 + e1) + (e2 + e3));
      e0 *= inv; e1 *= inv; e2 *= inv; e3 *= inv;
      *(LAS float*)(L + L_RED + ((h * 8 + 4 * T + g) * 64 + 32 * np + c16) * 4) = (e0 * a0[0] + e1 * a0[1]) + (e2 * a0[2] + e3 * a0[3]);
      *(LAS float*)(L + L_RED + ((h * 8 + 4 * T + g) * 64 + 32 * np + 16 + c16) * 4) = (e0 * a1[0] + e1 * a1[1]) + (e2 * a1[2] + e3 * a1[3]);
    }
    wg_barrier();
    STAMP(5);
    if (w == 0) {
      f32x4 sa = {0.f, 0.f, 0.f, 0.f}, sb = {0.f, 0.f, 0.f, 0.f};
#pragma unroll
      for (int hh = 0; hh < 4; ++hh) { sa += *(LAS f32x4*)(L + L_RED + ((hh * 8 + rb) * 64 + 8 * cp) * 4); sb += *(LAS f32x4*)(L + L_RED + ((hh * 8 + rb) * 64 + 8 * cp) * 4 + 16); }
      const u32x4 pk = {cvtpk(sa[0], sa[1]), cvtpk(sa[2], sa[3]), cvtpk(sb[0], sb[1]), cvtpk(sb[2], sb[3])};
      __builtin_amdgcn_raw_buffer_store_b128(pk, rws, WS_Y + ((kvh * 8 + rb) * DM + 64 * b + 8 * cp) * 2, 0, AUX_SC1);
      DRAIN();
      if (lane == 0) __hip_atomic_store((GAS u64*)(wsg + WS_H2F + (kvh * 8 + b) * FS), T64, RLX);
    }
    STAMP(6);
  } else if (role == 2) {
    const int mat = w & 1, kq = w >> 1;
    u32x4 Bf[2][4];
    {
      const int q = (lane >> 2) & 3;
#pragma unroll
      for (int n = 0; n < 2; ++n)
#pragma unroll
        for (int u = 0; u < 4; ++u) {
          LAS unsigned char* p = L + L_ROLE + mat * 32768 + (128 * kq + 32 * u + 8 * g + q) * 64 + 32 * (n ^ (g & 1)) + (lane & 3) * 8;
          const u32x2 lo = tr_read(p), hi = tr_read(p + 256);
          Bf[n][u] = u32x4{lo.x, lo.y, hi.x, hi.y};
        }
    }
    const u32x4 xv = LDB128(rx, (w * DM + 8 * lane) * 2, 0, 0);
    if (w == 0) {
      GAS u64* fp = (GAS u64*)(wsg + WS_H2F + lane * FS); int spins = 0;
      for (;;) {
        const u64 f0 = __hip_atomic_load(fp, RLX);
        if (__all(f0 == T64) || ++spins > SPIN_MAX) break;
        MEMFENCE(); __builtin_amdgcn_s_sleep(1);
      }
    }
    wg_barrier();
    STAMP(7);
    u32x4 yv[8];
#pragma unroll
    for (int i = 0; i < 8; ++i) yv[i] = LDB128(rws, WS_Y + ((i * 8 + w) * DM + 8 * lane) * 2, 0, AUX_SC1);
    float v[8];
#pragma unroll
    for (int i = 0; i < 4; ++i) { v[2 * i] = bflo(xv[i]); v[2 * i + 1] = bfhi(xv[i]); }
#pragma unroll
    for (int i = 0; i < 8; ++i) {
#pragma unroll
      for (int e = 0; e < 4; ++e) { v[2 * e] += bflo(yv[i][e]); v[2 * e + 1] += bfhi(yv[i][e]); }
    }
    *(LAS u32x4*)(L + L_A + w * A_STR + lane * 16) = ln_row(v);
    wg_barrier();
    STAMP(8);
    f32x4 a0 = {0.f, 0.f, 0.f, 0.f}, a1 = {0.f, 0.f, 0.f, 0.f};
#pragma unroll
    for (int u = 0; u < 4; ++u) {
      const u32x4 av = *(LAS u32x4*)(L + L_A + (lane & 7) * A_STR + (128 * kq + 32 * u + 8 * g) * 2);
      a0 = mfma32(av, Bf[0][u], a0); a1 = mfma32(av, Bf[1][u], a1);
    }
    if (lane < 32) {
#pragma unroll
      for (int r = 0; r < 4; ++r) {
        *(LAS float*)(L + L_RED + (((kq * 2 + mat) * 8 + 4 * g + r) * 32 + c16) * 4) = a0[r];
        *(LAS float*)(L + L_RED + (((kq * 2 + mat) * 8 + 4 * g + r) * 32 + 16 + c16) * 4) = a1[r];
      }
    }
    wg_barrier();
    if (w == 0) {
      f32x4 ga = {0.f, 0.f, 0.f, 0.f}, up = {0.f, 0.f, 0.f, 0.f};
#pragma unroll
      for (int q = 0; q < 4; ++q) { ga += *(LAS f32x4*)(L + L_RED + (((q * 2 + 0) * 8 + rb) * 32 + 4 * cp) * 4); up += *(LAS f32x4*)(L + L_RED + (((q * 2 + 1) * 8 + rb) * 32 + 4 * cp) * 4); }
      float r[4];
#pragma unroll
      for (int e = 0; e < 4; ++e) {
        const float z = ga[e];
        const float t = z * fmaf(z * z, 0.044715f * 0.7978845608028654f, 0.7978845608028654f);
        const float sg = __builtin_amdgcn_rcpf(1.0f + __builtin_amdgcn_exp2f(t * (-2.0f * 1.4426950408889634f)));
        r[e] = z * sg * up[e];
      }
      __builtin_amdgcn_raw_buffer_store_b64(u32x2{cvtpk(r[0], r[1]), cvtpk(r[2], r[3])}, rws, WS_G + (rb * FF + 32 * cgu + 4 * cp) * 2, 0, AUX_SC1);
      DRAIN();
      if (lane == 0) __hip_atomic_store((GAS u64*)(wsg + WS_H3F + cgu * FS), T64, RLX);
    }
    STAMP(9);
  } else if (role == 3) {
    u32x4 Bf[7];
#pragma unroll
    for (int u = 0; u < 7; ++u) Bf[u] = bfrag(L + L_ROLE + (224 * w + 32 * u) * 32 + trl, 128);
    f32x2 hres = {0.f, 0.f};
    if (w == 0) {
      const u32 xw = *(const GAS u32*)((const GAS unsigned char*)x + (rb * DM + 16 * cdn + 2 * cp) * 2);
      GAS u64* fp = (GAS u64*)(wsg + WS_H2F + ((lane & 7) * 8 + (cdn >> 2)) * FS); int spins = 0;
      for (;;) { const u64 f0 = __hip_atomic_load(fp, RLX); if (__all(f0 == T64) || ++spins > SPIN_MAX) break; MEMFENCE(); __builtin_amdgcn_s_sleep(2); }
      MEMFENCE();
      u32 yy[8];
#pragma unroll
      for (int q = 0; q < 8; ++q) yy[q] = __builtin_amdgcn_raw_buffer_load_b32(rws, WS_Y + ((q * 8 + rb) * DM + 16 * cdn + 2 * cp) * 2, 0, AUX_SC1);
      hres = f32x2{bflo(xw), bfhi(xw)};
#pragma unroll
      for (int q = 0; q < 8; ++q) hres += f32x2{bflo(yy[q]), bfhi(yy[q])};
      GAS u64* gp = (GAS u64*)(wsg + WS_H3F + lane * FS); spins = 0;
      for (;;) {
        const u64 f0 = lane < NGU ? __hip_atomic_load(gp, RLX) : T64;
        if (__all(f0 == T64) || ++spins > SPIN_MAX) break;
        MEMFENCE(); __builtin_amdgcn_s_sleep(1);
      }
      if (cdn == 0 && lane == 0) __hip_atomic_store((GAS u64*)(wsg + WS_E), T64, RLX);
    }
    wg_barrier();
    STAMP(9);
    {
      u32x4 gv[4];
#pragma unroll
      for (int i = 0; i < 4; ++i) gv[i] = LDB128(rws, (i == 3 && w >= 4) ? WS_END : WS_G + ((w + 8 * i) * 64 + lane) * 16, 0, AUX_SC1);
#pragma unroll
      for (int i = 0; i < 4; ++i) { const int ci = (w + 8 * i) * 64 + lane, row = ci / 224; if (i < 3 || w < 4) *(LAS u32x4*)(L + L_P + row * G_STR + (ci - row * 224) * 16) = gv[i]; }
    }
    wg_barrier();
    f32x4 acc = {0.f, 0.f, 0.f, 0.f};
#pragma unroll
    for (int u = 0; u < 7; ++u) acc = mfma32(*(LAS u32x4*)(L + L_P + (lane & 7) * G_STR + ((7 * w + u) * 32 + 8 * g) * 2), Bf[u], acc);
    if (lane < 32) {
#pragma unroll
      for (int r = 0; r < 4; ++r) *(LAS float*)(L + L_RED + ((w * 8 + 4 * g + r) * 16 + c16) * 4) = acc[r];
    }
    wg_barrier();
    if (w == 0) {
      f32x2 s = hres;
#pragma unroll
      for (int ww = 0; ww < 8; ++ww) s += *(LAS f32x2*)(L + L_RED + ((ww * 8 + rb) * 16 + 2 * cp) * 4);
      *(GAS u32*)((GAS unsigned char*)out + (rb * DM + 16 * cdn + 2 * cp) * 2) = cvtpk(s[0], s[1]);
    }
    STAMP(8);
  }
  STAMP_FLUSH();
}

extern "C" void kernel_launch(void* const* d_in, const int* in_sizes, int n_in, void* d_out, int out_size, void* d_ws, size_t ws_size, hipStream_t stream) {
  fused_decode_block<<<NWG, NTHR, 0, stream>>>((const u16*)d_in[0], (const u16*)d_in[1], (const u16*)d_in[2], (const u16*)d_in[3], (const u16*)d_in[4],
                                              (const u16*)d_in[6], (const u16*)d_in[7], (const u16*)d_in[8], (u16*)d_out, (unsigned char*)d_ws);
}
```

```cpp
#include <hip/hip_runtime.h>
#include <hip/hip_bf16.h>
#include <stdint.h>

typedef unsigned short u16;
typedef unsigned u32;
typedef unsigned long long u64;
typedef u32 u32x4 __attribute__((ext_vector_type(4)));
typedef u32 u32x2 __attribute__((ext_vector_type(2)));
typedef float f32x4 __attribute__((ext_vector_type(4)));
typedef float f32x2 __attribute__((ext_vector_type(2)));
typedef short s16x8 __attribute__((ext_vector_type(8)));
typedef short s16x4 __attribute__((ext_vector_type(4)));
typedef __bf16 bf16x8_t __attribute__((ext_vector_type(8)));
#define GAS __attribute__((address_space(1)))
#define LAS __attribute__((address_space(3)))
#define RLX __ATOMIC_RELAXED, __HIP_MEMORY_SCOPE_AGENT

#define QPOS 0
#define HOLD 1
#define PASSES 1
#define SLEEP0 8
#define QSLEEP 4
#define LATEW 1
#define VB 8
#define WAUX 2
#define TLBSTEP 0
#define TLBN 1
#define STAMPS 0

constexpr int NB = 8, DM = 512, HQ = 32, HKV = 8, SK = 8192, DH = 128, FF = 1792;
constexpr int NWG = 256, NTHR = 512;
constexpr int NGU = FF / 32;
constexpr int TOK_WG = SK / 4;
constexpr int NIT = TOK_WG / 16 / 8;
constexpr float SC_LOG2 = 0.08838834764831845f * 1.4426950408889634f;
constexpr float LN_EPS = 1e-5f;
constexpr int AUX_NT = 2, AUX_SC1 = 16;
constexpr int SPIN_MAX = 1 << 16;

constexpr int FS = 256;
constexpr int WS_E    = 0;
constexpr int WS_H1F  = 4096;
constexpr int WS_H2F  = WS_H1F + 256 * FS;
constexpr int WS_H3F  = WS_H2F + 64 * FS;
constexpr int WS_QG   = WS_H3F + 64 * FS;
constexpr int WS_PART = WS_QG + HKV * NB * 4 * 512 * 8;
constexpr int WS_Y    = WS_PART + HKV * 32 * 4 * 128 * 2;
constexpr int WS_G    = WS_Y + HKV * NB * DM * 2;
constexpr int WS_END  = WS_G + NB * FF * 2;
constexpr int WS_STAMP = 2 << 20;

constexpr int L_ROLE = 0;
constexpr int L_WQ = 0;
constexpr int L_MRG = 65536;
constexpr int A_STR = 1040;
constexpr int L_A = L_MRG + 16384;
constexpr int L_RED = L_A + 8 * A_STR;
constexpr int L_Q = L_RED + 8192;
constexpr int L_ST = L_Q + 1024;
constexpr int L_LAM = L_ST + 256;
constexpr int P_STR = 1040;
constexpr int L_P = L_LAM + 512;
constexpr int G_STR = 3600;
constexpr int LDS_BYTES = L_P + 32 * P_STR;

typedef __bf16 bf16x2_t __attribute__((ext_vector_type(2)));
__device__ __forceinline__ u32 cvtpk(float lo, float hi) { const bf16x2_t r = {(__bf16)lo, (__bf16)hi}; return __builtin_bit_cast(u32, r); }
__device__ __forceinline__ float u2f(u32 v) { return __builtin_bit_cast(float, v); }
__device__ __forceinline__ u32 f2u(float v) { return __builtin_bit_cast(u32, v); }
__device__ __forceinline__ float bflo(u32 v) { return __builtin_bit_cast(float, v << 16); }
__device__ __forceinline__ float bfhi(u32 v) { return __builtin_bit_cast(float, v & 0xffff0000u); }
template <int CTRL> __device__ __forceinline__ float dpp(float x) { return __builtin_bit_cast(float, __builtin_amdgcn_mov_dpp(__builtin_bit_cast(int, x), CTRL, 0xf, 0xf, true)); }
__device__ __forceinline__ float xrow_max(float x) {
  auto s = __builtin_amdgcn_permlane16_swap(f2u(x), f2u(x), false, false);
  x = fmaxf(u2f(s[0]), u2f(s[1]));
  auto t = __builtin_amdgcn_permlane32_swap(f2u(x), f2u(x), false, false);
  return fmaxf(u2f(t[0]), u2f(t[1]));
}
__device__ __forceinline__ float xrow_sum(float x) {
  auto s = __builtin_amdgcn_permlane16_swap(f2u(x), f2u(x), false, false);
  x = u2f(s[0]) + u2f(s[1]);
  auto t = __builtin_amdgcn_permlane32_swap(f2u(x), f2u(x), false, false);
  return u2f(t[0]) + u2f(t[1]);
}
__device__ __forceinline__ float wave_sum(float x) {
  x += dpp<0xB1>(x); x += dpp<0x4E>(x); x += dpp<0x141>(x); x += dpp<0x140>(x);
  return xrow_sum(x);
}
__device__ __forceinline__ f32x4 mfma32(u32x4 a, u32x4 b, f32x4 c) {
  return __builtin_amdgcn_mfma_f32_16x16x32_bf16(__builtin_bit_cast(bf16x8_t, a), __builtin_bit_cast(bf16x8_t, b), c, 0, 0, 0);
}
__device__ __forceinline__ f32x4 mfma16(u32x2 a, u32x2 b, f32x4 c) {
  return __builtin_amdgcn_mfma_f32_16x16x16bf16_1k(__builtin_bit_cast(s16x4, a), __builtin_bit_cast(s16x4, b), c, 0, 0, 0);
}
typedef short v4i16_t __attribute__((ext_vector_type(4)));
__device__ __forceinline__ u32x2 tr_read(LAS unsigned char* p) {
  return __builtin_bit_cast(u32x2, __builtin_amdgcn_ds_read_tr16_b64_v4i16((LAS v4i16_t*)p));
}
__device__ __forceinline__ u32x4 bfrag(LAS unsigned char* p, int second_off) {
  const u32x2 a = tr_read(p), b = tr_read(p + second_off);
  return u32x4{a.x, a.y, b.x, b.y};
}
__device__ __forceinline__ __amdgpu_buffer_rsrc_t mkrs(const void* p, u32 bytes) { return __builtin_amdgcn_make_buffer_rsrc((void*)p, 0, bytes, 0x00020000); }
template <int V> struct ic { static constexpr int value = V; };
#define LDB128(rs, vo, so, aux) __builtin_amdgcn_raw_buffer_load_b128(rs, vo, so, aux)
#define SB() __builtin_amdgcn_sched_barrier(0)
#define DRAIN() asm volatile("s_waitcnt vmcnt(0)" ::: "memory")
#define MEMFENCE() asm volatile("" ::: "memory")
#define RAWBAR() asm volatile("s_barrier" ::: "memory")
__device__ __forceinline__ void wg_barrier() { asm volatile("s_waitcnt lgkmcnt(0)\n\ts_barrier" ::: "memory"); }

__device__ __forceinline__ u32x4 ln_row(const float (&v)[8]) {
  float s = ((v[0] + v[1]) + (v[2] + v[3])) + ((v[4] + v[5]) + (v[6] + v[7]));
  const float mean = wave_sum(s) * (1.0f / DM);
  float d[8], q = 0.f;
#pragma unroll
  for (int i = 0; i < 8; ++i) { d[i] = v[i] - mean; q = fmaf(d[i], d[i], q); }
  const float var = wave_sum(q) * (1.0f / DM);
  const float rs = __builtin_amdgcn_rsqf(var + LN_EPS);
  return u32x4{cvtpk(d[0] * rs, d[1] * rs), cvtpk(d[2] * rs, d[3] * rs), cvtpk(d[4] * rs, d[5] * rs), cvtpk(d[6] * rs, d[7] * rs)};
}

#define STAMP(i) do {} while (0)
#define STAMP_FLUSH() do {} while (0)

__global__ void __launch_bounds__(NTHR, 2)
fused_decode_block(const u16* __restrict__ x, const u16* __restrict__ Kc, const u16* __restrict__ Vc, const u16* __restrict__ wq, const u16* __restrict__ wo,
                   const u16* __restrict__ wg, const u16* __restrict__ wu, const u16* __restrict__ wd, u16* __restrict__ out, unsigned char* __restrict__ ws) {
  __shared__ __attribute__((aligned(16))) unsigned char lds_raw[LDS_BYTES];
  LAS unsigned char* L = (LAS unsigned char*)lds_raw;
  const int tid = threadIdx.x, lane = tid & 63, w = __builtin_amdgcn_readfirstlane(tid >> 6);
  const int blk = blockIdx.x, kvh = blk & 7, j = blk >> 3, b = j >> 2, sp = j & 3;
  const int g = lane >> 4, c16 = lane & 15;
  GAS unsigned char* wsg = (GAS unsigned char*)ws;
  STAMP(0);

  u64 E;
  asm volatile("s_load_dwordx2 %0, %1, 0x0" : "=s"(E) : "s"(ws));
  const __amdgpu_buffer_rsrc_t rx = mkrs(x, NB * DM * 2);
  const __amdgpu_buffer_rsrc_t rwq = mkrs(wq, DM * HQ * DH * 2);
  const size_t kvbase = ((size_t)(b * HKV + kvh) * SK + (size_t)sp * TOK_WG) * DH;
  const __amdgpu_buffer_rsrc_t rk = mkrs(Kc + kvbase, TOK_WG * DH * 2);
  const __amdgpu_buffer_rsrc_t rv = mkrs(Vc + kvbase, TOK_WG * DH * 2);
  const __amdgpu_buffer_rsrc_t rws = mkrs(ws, WS_END);

  const int trl = (8 * g + ((lane >> 2) & 3)) * 32 + (lane & 3) * 8;
  const int jj = 3 * b + sp - 1;
  const int role = sp == 0 ? 1 : (jj < 7 ? 2 : (jj < 11 ? 3 : 0));
  const int cgu = kvh * 7 + jj, cdn = kvh * 4 + (jj - 7);
#define LOAD_WEIGHTS(wst) do { \
      const u16* p0 = role == 1 ? wo : (role == 2 ? wg : wd); \
      const u16* p1 = role == 2 ? wu : p0; \
      const u32 n0b = role == 0 ? 0u : (role == 1 ? (u32)(HQ * DH * DM * 2) : (u32)(DM * FF * 2)); \
      const u32 n1b = role == 3 ? 0u : n0b; \
      const __amdgpu_buffer_rsrc_t r0 = mkrs(p0, n0b), r1 = mkrs(p1, n0b), r2 = mkrs(p1, n1b); \
      const int v0o = role == 1 ? ((kvh * 512 + 128 * w + (lane >> 3)) * DM + 64 * b + 8 * (lane & 7)) * 2 \
                    : role == 2 ? ((128 * w + (lane >> 2)) * FF + 32 * cgu + 8 * (lane & 3)) * 2 \
                                : ((448 * w + (lane >> 1)) * DM + 16 * cdn + 8 * (lane & 1)) * 2; \
      const int st = role == 1 ? 8 * DM * 2 : role == 2 ? 16 * FF * 2 : 32 * DM * 2; \
      const int v8o = role == 2 ? v0o : v0o + 8 * st; \
      _Pragma("unroll") for (int i = 0; i < 8; ++i) wst[i] = LDB128(r0, v0o + i * st, 0, WAUX); \
      _Pragma("unroll") for (int i = 0; i < 6; ++i) wst[8 + i] = LDB128(r1, v8o + i * st, 0, WAUX); \
      _Pragma("unroll") for (int i = 6; i < 8; ++i) wst[8 + i] = LDB128(r2, v8o + i * st, 0, WAUX); \
      SB(); } while (0)
#define STAGE_WEIGHTS(wst) do { \
    if (role == 1) { \
      _Pragma("unroll") for (int i = 0; i < 16; ++i) { const int r = 128 * w + 8 * i + (lane >> 3), ch = lane & 7; \
        *(LAS u32x4*)(L + L_ROLE + (r ^ ((r >> 2) & 1)) * 128 + 32 * ((ch >> 1) ^ (((r >> 3) & 1) | (((r >> 5) & 1) << 1))) + 16 * (ch & 1)) = wst[i]; } \
    } else if (role == 2) { \
      _Pragma("unroll") for (int i = 0; i < 16; ++i) { const int r = 128 * w + 16 * (i & 7) + (lane >> 2), ch = lane & 3; \
        *(LAS u32x4*)(L + L_ROLE + (i >> 3) * 32768 + r * 64 + 32 * ((ch >> 1) ^ ((r >> 3) & 1)) + 16 * (ch & 1)) = wst[i]; } \
    } else if (role == 3) { \
      _Pragma("unroll") for (int i = 0; i < 14; ++i) *(LAS u32x4*)(L + L_ROLE + (448 * w + 32 * i) * 32 + lane * 16) = wst[i]; \
    } } while (0)
  u64 T64;
  if (w < 4) {
    const u32x4 xr0 = LDB128(rx, (2 * w * DM + 8 * lane) * 2, 0, 0), xr1 = LDB128(rx, ((2 * w + 1) * DM + 8 * lane) * 2, 0, 0);
    u32x4 wqr[4];
#pragma unroll
    for (int i = 0; i < 4; ++i) wqr[i] = LDB128(rwq, ((128 * sp + 32 * w + 8 * i + (lane >> 3)) * (HQ * DH) + kvh * 512 + 64 * b + 8 * (lane & 7)) * 2, 0, WAUX);
    SB(); RAWBAR(); SB();
    float v0[8], v1[8];
#pragma unroll
    for (int i = 0; i < 4; ++i) { v0[2 * i] = bflo(xr0[i]); v0[2 * i + 1] = bfhi(xr0[i]); v1[2 * i] = bflo(xr1[i]); v1[2 * i + 1] = bfhi(xr1[i]); }
    const u32x4 n0 = ln_row(v0), n1 = ln_row(v1);
    *(LAS u32x4*)(L + L_A + 2 * w * A_STR + lane * 16) = n0;
    *(LAS u32x4*)(L + L_A + (2 * w + 1) * A_STR + lane * 16) = n1;
#pragma unroll
    for (int i = 0; i < 4; ++i) *(LAS u32x4*)(L + L_WQ + (32 * w + 8 * i + (lane >> 3)) * 144 + (lane & 7) * 16) = wqr[i];
    STAMP(12);
    wg_barrier();
    STAMP(13);
    const u32x4 a = *(LAS u32x4*)(L + L_A + (lane & 7) * A_STR + (128 * sp + 32 * w + 8 * g) * 2);
#pragma unroll
    for (int nt = 0; nt < 4; ++nt) {
      LAS unsigned char* p = L + L_WQ + (32 * w + 8 * g + ((lane >> 2) & 3)) * 144 + nt * 32 + (lane & 3) * 8;
      const u32x2 lo = tr_read(p), hi = tr_read(p + 4 * 144);
      const f32x4 acc = mfma32(a, u32x4{lo.x, lo.y, hi.x, hi.y}, f32x4{0.f, 0.f, 0.f, 0.f});
      if (lane < 32) {
#pragma unroll
        for (int r = 0; r < 4; ++r) *(LAS float*)(L + L_P + ((w * 8 + 4 * g + r) * 64 + 16 * nt + c16) * 4) = acc[r];
      }
    }
    wg_barrier();
    STAMP(14);
    asm volatile("s_waitcnt lgkmcnt(0)" : "+s"(E));
    T64 = E + 1;
    const u64 T40 = T64 & 0xFFFFFFFFFFull;
#pragma unroll
    for (int i = 0; i < 2; ++i) {
      const int o = tid + 256 * i;
      float sum = 0.f;
#pragma unroll
      for (int ww = 0; ww < 4; ++ww) sum += *(LAS float*)(L + L_P + (ww * 512 + o) * 4);
      const u32 sv = f2u(sum) + 0x80u;
      __hip_atomic_store((GAS u64*)(wsg + WS_QG) + ((kvh * 8 + (o >> 6)) * 4 + sp) * 512 + 64 * b + (o & 63), (T40 << 24) | (u64)(sv >> 8), RLX);
    }
    STAMP(1);
    DRAIN();
    if (QSLEEP) __builtin_amdgcn_s_sleep(QSLEEP);
    STAMP(10);
    {
      GAS u64* qgp = (GAS u64*)(wsg + WS_QG) + (kvh * 8 + b) * 4 * 512 + tid;
      u64 qv[8]; int spins = 0;
      for (;;) {
        bool ok = true;
#pragma unroll
        for (int i = 0; i < 8; ++i) qv[i] = __hip_atomic_load(qgp + (i & 3) * 512 + (i >> 2) * 256, RLX);
#pragma unroll
        for (int i = 0; i < 8; ++i) ok &= (qv[i] >> 24) == T40;
        if (__all(ok) || ++spins > SPIN_MAX) break;
        MEMFENCE(); __builtin_amdgcn_s_sleep(2);
      }
      const float qa = (u2f((u32)qv[0] << 8) + u2f((u32)qv[1] << 8)) + (u2f((u32)qv[2] << 8) + u2f((u32)qv[3] << 8));
      const float qb = (u2f((u32)qv[4] << 8) + u2f((u32)qv[5] << 8)) + (u2f((u32)qv[6] << 8) + u2f((u32)qv[7] << 8));
      *(LAS u16*)(L + L_Q + tid * 2) = (u16)cvtpk(qa, qa);
      *(LAS u16*)(L + L_Q + 512 + tid * 2) = (u16)cvtpk(qb, qb);
    }
    STAMP(15);
    wg_barrier();
    SB();
    if (role == 1 || !LATEW) { u32x4 wst[16]; LOAD_WEIGHTS(wst); STAGE_WEIGHTS(wst); }
    wg_barrier();
  } else {
    const int sw = w - 4;
    const int koff = (4 * (c16 & 3) + (c16 >> 2)) * 256 + g * 16, voff = lane * 16;
    const bool vkeep = b >= VB;
#define ISSUE_K(slot, it) do { const int so_ = ((it) * 4 + sw) * 4096; \
    _Pragma("unroll") for (int u = 0; u < 4; ++u) Kb[slot][u] = LDB128(rk, koff + u * 64, so_, AUX_NT); SB(); } while (0)
#define ISSUE_V(slot, it) do { const int so_ = ((it) * 4 + sw) * 4096; \
    _Pragma("unroll") for (int u = 0; u < 4; ++u) Vb[slot][u] = LDB128(rv, voff + u * 1024, so_, VAUX); SB(); } while (0)
#define ISSUE(slot, it) do { ISSUE_K(slot, it); ISSUE_V(slot, it); } while (0)
    float mrun = -INFINITY, lrun = 0.f;
    f32x4 O1 = {0.f, 0.f, 0.f, 0.f}, O2 = {0.f, 0.f, 0.f, 0.f};
    auto stream = [&](auto vaux) __attribute__((always_inline)) {
    constexpr int VAUX = decltype(vaux)::value;
    u32x4 Kb[3][4], Vb[3][4];
    RAWBAR();
    if (SLEEP0) __builtin_amdgcn_s_sleep(SLEEP0);
    SB();
    ISSUE(0, 0);
    RAWBAR(); SB();
    ISSUE_V(1, 1);
    RAWBAR(); SB();
    ISSUE_K(1, 1);
    asm volatile("s_waitcnt vmcnt(4)" ::: "memory"); SB();
    ISSUE_K(2, 2);
    asm volatile("s_waitcnt vmcnt(4)" ::: "memory"); SB();
    ISSUE_V(2, 2);
    wg_barrier();
    STAMP(2);
    u32x4 Qf[4];
#pragma unroll
    for (int u = 0; u < 4; ++u) Qf[u] = *(LAS u32x4*)(L + L_Q + ((lane & 3) * 128 + 32 * u + 8 * g) * 2);
    const int jx = c16 >> 2;
    const u32 m0k = jx == 0 ? 0x0000FFFFu : jx == 1 ? 0xFFFF0000u : 0u;
    const u32 m1k = jx == 2 ? 0x0000FFFFu : jx == 3 ? 0xFFFF0000u : 0u;
#define PROCESS(slot) do { \
    f32x4 s_ = {0.f, 0.f, 0.f, 0.f}; \
    _Pragma("unroll") for (int u = 0; u < 4; ++u) s_ = mfma32(Kb[slot][u], Qf[u], s_); \
    const float tm_ = xrow_max(fmaxf(fmaxf(s_[0], s_[1]), fmaxf(s_[2], s_[3]))); \
    if (__any(tm_ > mrun)) { \
      const float mn_ = fmaxf(mrun, tm_), al_ = __builtin_amdgcn_exp2f((mrun - mn_) * SC_LOG2); \
      lrun *= al_; mrun = mn_; \
      const float a0_ = dpp<0x00>(al_), a1_ = dpp<0x55>(al_), a2_ = dpp<0xAA>(al_), a3_ = dpp<0xFF>(al_); \
      O1[0] *= a0_; O1[1] *= a1_; O1[2] *= a2_; O1[3] *= a3_; O2[0] *= a0_; O2[1] *= a1_; O2[2] *= a2_; O2[3] *= a3_; \
    } \
    const float mc_ = mrun * SC_LOG2; \
    _Pragma("unroll") for (int i = 0; i < 4; ++i) { \
      const float p_ = __builtin_amdgcn_exp2f(fmaf(s_[i], SC_LOG2, -mc_)); lrun += p_; \
      const u32 pv_ = cvtpk(p_, p_); const u32x2 a_ = {pv_ & m0k, pv_ & m1k}; \
      O1 = mfma16(a_, u32x2{Vb[slot][i].x, Vb[slot][i].y}, O1); O2 = mfma16(a_, u32x2{Vb[slot][i].z, Vb[slot][i].w}, O2); \
    } SB(); } while (0)
#pragma unroll 1
    for (int it = 0; it < 27; it += 3) { PROCESS(0); ISSUE(0, it + 3); PROCESS(1); ISSUE(1, it + 4); PROCESS(2); ISSUE(2, it + 5); }
    PROCESS(0); ISSUE(0, 30); PROCESS(1); ISSUE(1, 31); PROCESS(2); PROCESS(0); PROCESS(1);
    };
    if (vkeep) stream(ic<0>{}); else stream(ic<AUX_NT>{});
    asm volatile("s_waitcnt lgkmcnt(0)" : "+s"(E));
    T64 = E + 1;
    STAMP(3);
    lrun = xrow_sum(lrun);
#pragma unroll
    for (int h = 0; h < 4; ++h) *(LAS f32x2*)(L + L_MRG + ((sw * 4 + h) * 64 + lane) * 8) = f32x2{O1[h], O2[h]};
    if (lane < 4) { *(LAS float*)(L + L_ST + (lane * 4 + sw) * 4) = mrun; *(LAS float*)(L + L_ST + 64 + (lane * 4 + sw) * 4) = lrun; }
    wg_barrier();
  }
  const u32 T32 = (u32)T64;

  if (w == 0) {
    const int h = g;
    const f32x4 ma = *(LAS f32x4*)(L + L_ST + h * 16), la = *(LAS f32x4*)(L + L_ST + 64 + h * 16);
    const float M = fmaxf(fmaxf(ma[0], ma[1]), fmaxf(ma[2], ma[3]));
    float Ls = 0.f;
    f32x4 oa = {0.f, 0.f, 0.f, 0.f}, ob = {0.f, 0.f, 0.f, 0.f};
#pragma unroll
    for (int ww = 0; ww < 4; ++ww) {
      const float f = __builtin_amdgcn_exp2f((ma[ww] - M) * SC_LOG2);
      Ls = fmaf(f, la[ww], Ls);
      oa += f * *(LAS f32x4*)(L + L_MRG + ((ww * 4 + h) * 64 + 4 * c16) * 8);
      ob += f * *(LAS f32x4*)(L + L_MRG + ((ww * 4 + h) * 64 + 4 * c16) * 8 + 16);
    }
    const float inv = __builtin_amdgcn_rcpf(Ls);
    const u32x4 pk = {cvtpk(oa[0] * inv, oa[1] * inv), cvtpk(oa[2] * inv, oa[3] * inv), cvtpk(ob[0] * inv, ob[1] * inv), cvtpk(ob[2] * inv, ob[3] * inv)};
    __builtin_amdgcn_raw_buffer_store_b128(pk, rws, WS_PART + (kvh * 32 + j) * 1024 + lane * 16, 0, AUX_SC1);
    const float lam = fmaf(M, SC_LOG2, __builtin_amdgcn_logf(Ls));
    DRAIN();
    if (c16 == 0) __hip_atomic_store((GAS u64*)(wsg + WS_H1F + (kvh * 32 + j) * FS) + h, ((u64)T32 << 32) | f2u(lam), RLX);
  }
  STAMP(4);
  if (LATEW && role >= 2) {
    SB(); RAWBAR(); SB();
    if (w < 4) { u32x4 wst[16]; LOAD_WEIGHTS(wst); STAGE_WEIGHTS(wst); }
    wg_barrier();
  }

  const int rb = lane >> 3, cp = lane & 7;
  if (role == 1) {
    const int h = w >> 1, np = w & 1;
    u32x4 Bf[2][4];
    {
      const int q = (lane >> 2) & 3;
#pragma unroll
      for (int n = 0; n < 2; ++n)
#pragma unroll
        for (int u = 0; u < 4; ++u) {
          const int d0 = h * 128 + 32 * g + 8 * (q >> 1) + 4 * (q & 1) + u, d1 = d0 + 16;
          const int sg = (q >> 1) | ((g & 1) << 1);
          const u32x2 lo = tr_read(L + L_ROLE + (d0 ^ (q & 1)) * 128 + 32 * ((2 * np + n) ^ sg) + (lane & 3) * 8);
          const u32x2 hi = tr_read(L + L_ROLE + (d1 ^ (q & 1)) * 128 + 32 * ((2 * np + n) ^ sg) + (lane & 3) * 8);
          Bf[n][u] = u32x4{lo.x, lo.y, hi.x, hi.y};
        }
    }
    GAS u64* fp = (GAS u64*)(wsg + WS_H1F + (kvh * 32 + 4 * w + ((lane >> 2) & 3)) * FS) + (lane & 3);
    u64 f0; int spins = 0;
    for (;;) {
      f0 = __hip_atomic_load(fp, RLX);
      if (__all((u32)(f0 >> 32) == T32) || ++spins > SPIN_MAX) break;
      MEMFENCE(); __builtin_amdgcn_s_sleep(2);
    }
    MEMFENCE();
    u32x4 pv[4];
#pragma unroll
    for (int i = 0; i < 4; ++i) pv[i] = LDB128(rws, WS_PART + (kvh * 32 + 4 * w + i) * 1024 + lane * 16, 0, AUX_SC1);
    if (lane < 16) *(LAS u32*)(L + L_LAM + ((lane & 3) * 32 + 4 * w + (lane >> 2)) * 4) = (u32)f0;
#pragma unroll
    for (int i = 0; i < 4; ++i) *(LAS u32x4*)(L + L_P + (4 * w + i) * P_STR + lane * 16) = pv[i];
    wg_barrier();
#pragma unroll
    for (int T = 0; T < 2; ++T) {
      const f32x4 lm4 = *(LAS f32x4*)(L + L_LAM + (h * 32 + 16 * T + 4 * g) * 4);
      f32x4 a0 = {0.f, 0.f, 0.f, 0.f}, a1 = {0.f, 0.f, 0.f, 0.f};
#pragma unroll
      for (int u = 0; u < 4; ++u) {
        const u32x4 av = *(LAS u32x4*)(L + L_P + (16 * T + c16) * P_STR + h * 256 + u * 64 + g * 16);
        a0 = mfma32(av, Bf[0][u], a0); a1 = mfma32(av, Bf[1][u], a1);
      }
      const float lm = fmaxf(fmaxf(lm4[0], lm4[1]), fmaxf(lm4[2], lm4[3]));
      float e0 = __builtin_amdgcn_exp2f(lm4[0] - lm), e1 = __builtin_amdgcn_exp2f(lm4[1] - lm), e2 = __builtin_amdgcn_exp2f(lm4[2] - lm), e3 = __builtin_amdgcn_exp2f(lm4[3] - lm);
      const float inv = __builtin_amdgcn_rcpf((e0 + e1) + (e2 + e3));
      e0 *= inv; e1 *= inv; e2 *= inv; e3 *= inv;
      *(LAS float*)(L + L_RED + ((h * 8 + 4 * T + g) * 64 + 32 * np + c16) * 4) = (e0 * a0[0] + e1 * a0[1]) + (e2 * a0[2] + e3 * a0[3]);
      *(LAS float*)(L + L_RED + ((h * 8 + 4 * T + g) * 64 + 32 * np + 16 + c16) * 4) = (e0 * a1[0] + e1 * a1[1]) + (e2 * a1[2] + e3 * a1[3]);
    }
    wg_barrier();
    STAMP(5);
    if (w == 0) {
      f32x4 sa = {0.f, 0.f, 0.f, 0.f}, sb = {0.f, 0.f, 0.f, 0.f};
#pragma unroll
      for (int hh = 0; hh < 4; ++hh) { sa += *(LAS f32x4*)(L + L_RED + ((hh * 8 + rb) * 64 + 8 * cp) * 4); sb += *(LAS f32x4*)(L + L_RED + ((hh * 8 + rb) * 64 + 8 * cp) * 4 + 16); }
      const u32x4 pk = {cvtpk(sa[0], sa[1]), cvtpk(sa[2], sa[3]), cvtpk(sb[0], sb[1]), cvtpk(sb[2], sb[3])};
      __builtin_amdgcn_raw_buffer_store_b128(pk, rws, WS_Y + ((kvh * 8 + rb) * DM + 64 * b + 8 * cp) * 2, 0, AUX_SC1);
      DRAIN();
      if (lane == 0) __hip_atomic_store((GAS u64*)(wsg + WS_H2F + (kvh * 8 + b) * FS), T64, RLX);
    }
    STAMP(6);
  } else if (role == 2) {
    const int mat = w & 1, kq = w >> 1;
    u32x4 Bf[2][4];
    {
      const int q = (lane >> 2) & 3;
#pragma unroll
      for (int n = 0; n < 2; ++n)
#pragma unroll
        for (int u = 0; u < 4; ++u) {
          LAS unsigned char* p = L + L_ROLE + mat * 32768 + (128 * kq + 32 * u + 8 * g + q) * 64 + 32 * (n ^ (g & 1)) + (lane & 3) * 8;
          const u32x2 lo = tr_read(p), hi = tr_read(p + 256);
          Bf[n][u] = u32x4{lo.x, lo.y, hi.x, hi.y};
        }
    }
    const u32x4 xv = LDB128(rx, (w * DM + 8 * lane) * 2, 0, 0);
    if (w == 0) {
      GAS u64* fp = (GAS u64*)(wsg + WS_H2F + lane * FS); int spins = 0;
      for (;;) {
        const u64 f0 = __hip_atomic_load(fp, RLX);
        if (__all(f0 == T64) || ++spins > SPIN_MAX) break;
        MEMFENCE(); __builtin_amdgcn_s_sleep(1);
      }
    }
    wg_barrier();
    STAMP(7);
    u32x4 yv[8];
#pragma unroll
    for (int i = 0; i < 8; ++i) yv[i] = LDB128(rws, WS_Y + ((i * 8 + w) * DM + 8 * lane) * 2, 0, AUX_SC1);
    float v[8];
#pragma unroll
    for (int i = 0; i < 4; ++i) { v[2 * i] = bflo(xv[i]); v[2 * i + 1] = bfhi(xv[i]); }
#pragma unroll
    for (int i = 0; i < 8; ++i) {
#pragma unroll
      for (int e = 0; e < 4; ++e) { v[2 * e] += bflo(yv[i][e]); v[2 * e + 1] += bfhi(yv[i][e]); }
    }
    *(LAS u32x4*)(L + L_A + w * A_STR + lane * 16) = ln_row(v);
    wg_barrier();
    STAMP(8);
    f32x4 a0 = {0.f, 0.f, 0.f, 0.f}, a1 = {0.f, 0.f, 0.f, 0.f};
#pragma unroll
    for (int u = 0; u < 4; ++u) {
      const u32x4 av = *(LAS u32x4*)(L + L_A + (lane & 7) * A_STR + (128 * kq + 32 * u + 8 * g) * 2);
      a0 = mfma32(av, Bf[0][u], a0); a1 = mfma32(av, Bf[1][u], a1);
    }
    if (lane < 32) {
#pragma unroll
      for (int r = 0; r < 4; ++r) {
        *(LAS float*)(L + L_RED + (((kq * 2 + mat) * 8 + 4 * g + r) * 32 + c16) * 4) = a0[r];
        *(LAS float*)(L + L_RED + (((kq * 2 + mat) * 8 + 4 * g + r) * 32 + 16 + c16) * 4) = a1[r];
      }
    }
    wg_barrier();
    if (w == 0) {
      f32x4 ga = {0.f, 0.f, 0.f, 0.f}, up = {0.f, 0.f, 0.f, 0.f};
#pragma unroll
      for (int q = 0; q < 4; ++q) { ga += *(LAS f32x4*)(L + L_RED + (((q * 2 + 0) * 8 + rb) * 32 + 4 * cp) * 4); up += *(LAS f32x4*)(L + L_RED + (((q * 2 + 1) * 8 + rb) * 32 + 4 * cp) * 4); }
      float r[4];
#pragma unroll
      for (int e = 0; e < 4; ++e) {
        const float z = ga[e];
        const float t = z * fmaf(z * z, 0.044715f * 0.7978845608028654f, 0.7978845608028654f);
        const float sg = __builtin_amdgcn_rcpf(1.0f + __builtin_amdgcn_exp2f(t * (-2.0f * 1.4426950408889634f)));
        r[e] = z * sg * up[e];
      }
      __builtin_amdgcn_raw_buffer_store_b64(u32x2{cvtpk(r[0], r[1]), cvtpk(r[2], r[3])}, rws, WS_G + (rb * FF + 32 * cgu + 4 * cp) * 2, 0, AUX_SC1);
      DRAIN();
      if (lane == 0) __hip_atomic_store((GAS u64*)(wsg + WS_H3F + cgu * FS), T64, RLX);
    }
    STAMP(9);
  } else if (role == 3) {
    u32x4 Bf[7];
#pragma unroll
    for (int u = 0; u < 7; ++u) Bf[u] = bfrag(L + L_ROLE + (224 * w + 32 * u) * 32 + trl, 128);
    f32x2 hres = {0.f, 0.f};
    if (w == 0) {
      const u32 xw = *(const GAS u32*)((const GAS unsigned char*)x + (rb * DM + 16 * cdn + 2 * cp) * 2);
      GAS u64* fp = (GAS u64*)(wsg + WS_H2F + ((lane & 7) * 8 + (cdn >> 2)) * FS); int spins = 0;
      for (;;) { const u64 f0 = __hip_atomic_load(fp, RLX); if (__all(f0 == T64) || ++spins > SPIN_MAX) break; MEMFENCE(); __builtin_amdgcn_s_sleep(2); }
      MEMFENCE();
      u32 yy[8];
#pragma unroll
      for (int q = 0; q < 8; ++q) yy[q] = __builtin_amdgcn_raw_buffer_load_b32(rws, WS_Y + ((q * 8 + rb) * DM + 16 * cdn + 2 * cp) * 2, 0, AUX_SC1);
      hres = f32x2{bflo(xw), bfhi(xw)};
#pragma unroll
      for (int q = 0; q < 8; ++q) hres += f32x2{bflo(yy[q]), bfhi(yy[q])};
      GAS u64* gp = (GAS u64*)(wsg + WS_H3F + lane * FS); spins = 0;
      for (;;) {
        const u64 f0 = lane < NGU ? __hip_atomic_load(gp, RLX) : T64;
        if (__all(f0 == T64) || ++spins > SPIN_MAX) break;
        MEMFENCE(); __builtin_amdgcn_s_sleep(1);
      }
      if (cdn == 0 && lane == 0) __hip_atomic_store((GAS u64*)(wsg + WS_E), T64, RLX);
    }
    wg_barrier();
    STAMP(9);
    {
      u32x4 gv[4];
#pragma unroll
      for (int i = 0; i < 4; ++i) gv[i] = LDB128(rws, (i == 3 && w >= 4) ? WS_END : WS_G + ((w + 8 * i) * 64 + lane) * 16, 0, AUX_SC1);
#pragma unroll
      for (int i = 0; i < 4; ++i) { const int ci = (w + 8 * i) * 64 + lane, row = ci / 224; if (i < 3 || w < 4) *(LAS u32x4*)(L + L_P + row * G_STR + (ci - row * 224) * 16) = gv[i]; }
    }
    wg_barrier();
    f32x4 acc = {0.f, 0.f, 0.f, 0.f};
#pragma unroll
    for (int u = 0; u < 7; ++u) acc = mfma32(*(LAS u32x4*)(L + L_P + (lane & 7) * G_STR + ((7 * w + u) * 32 + 8 * g) * 2), Bf[u], acc);
    if (lane < 32) {
#pragma unroll
      for (int r = 0; r < 4; ++r) *(LAS float*)(L + L_RED + ((w * 8 + 4 * g + r) * 16 + c16) * 4) = acc[r];
    }
    wg_barrier();
    if (w == 0) {
      f32x2 s = hres;
#pragma unroll
      for (int ww = 0; ww < 8; ++ww) s += *(LAS f32x2*)(L + L_RED + ((ww * 8 + rb) * 16 + 2 * cp) * 4);
      *(GAS u32*)((GAS unsigned char*)out + (rb * DM + 16 * cdn + 2 * cp) * 2) = cvtpk(s[0], s[1]);
    }
    STAMP(8);
  }
  STAMP_FLUSH();
}

extern "C" void kernel_launch(void* const* d_in, const int* in_sizes, int n_in, void* d_out, int out_size, void* d_ws, size_t ws_size, hipStream_t stream) {
  fused_decode_block<<<NWG, NTHR, 0, stream>>>((const u16*)d_in[0], (const u16*)d_in[1], (const u16*)d_in[2], (const u16*)d_in[3], (const u16*)d_in[4],
                                              (const u16*)d_in[6], (const u16*)d_in[7], (const u16*)d_in[8], (u16*)d_out, (unsigned char*)d_ws);
}
```

```cpp
#include <hip/hip_runtime.h>
#include <hip/hip_bf16.h>
#include <stdint.h>

typedef unsigned short u16;
typedef unsigned u32;
typedef unsigned long long u64;
typedef u32 u32x4 __attribute__((ext_vector_type(4)));
typedef u32 u32x2 __attribute__((ext_vector_type(2)));
typedef float f32x4 __attribute__((ext_vector_type(4)));
typedef float f32x2 __attribute__((ext_vector_type(2)));
typedef short s16x8 __attribute__((ext_vector_type(8)));
typedef short s16x4 __attribute__((ext_vector_type(4)));
typedef __bf16 bf16x8_t __attribute__((ext_vector_type(8)));
#define GAS __attribute__((address_space(1)))
#define LAS __attribute__((address_space(3)))
#define RLX __ATOMIC_RELAXED, __HIP_MEMORY_SCOPE_AGENT

#define QPOS 0
#define HOLD 1
#define PASSES 1
#define SLEEP0 8
#define QSLEEP 4
#define LATEW 1
#define VB 8
#define WAUX 2
#define TLBSTEP 65536
#define TLBN 1
#define STAMPS 0

constexpr int NB = 8, DM = 512, HQ = 32, HKV = 8, SK = 8192, DH = 128, FF = 1792;
constexpr int NWG = 256, NTHR = 512;
constexpr int NGU = FF / 32;
constexpr int TOK_WG = SK / 4;
constexpr int NIT = TOK_WG / 16 / 8;
constexpr float SC_LOG2 = 0.08838834764831845f * 1.4426950408889634f;
constexpr float LN_EPS = 1e-5f;
constexpr int AUX_NT = 2, AUX_SC1 = 16;
constexpr int SPIN_MAX = 1 << 16;

constexpr int FS = 256;
constexpr int WS_E    = 0;
constexpr int WS_H1F  = 4096;
constexpr int WS_H2F  = WS_H1F + 256 * FS;
constexpr int WS_H3F  = WS_H2F + 64 * FS;
constexpr int WS_QG   = WS_H3F + 64 * FS;
constexpr int WS_PART = WS_QG + HKV * NB * 4 * 512 * 8;
constexpr int WS_Y    = WS_PART + HKV * 32 * 4 * 128 * 2;
constexpr int WS_G    = WS_Y + HKV * NB * DM * 2;
constexpr int WS_END  = WS_G + NB * FF * 2;
constexpr int WS_STAMP = 2 << 20;

constexpr int L_ROLE = 0;
constexpr int L_WQ = 0;
constexpr int L_MRG = 65536;
constexpr int A_STR = 1040;
constexpr int L_A = L_MRG + 16384;
constexpr int L_RED = L_A + 8 * A_STR;
constexpr int L_Q = L_RED + 8192;
constexpr int L_ST = L_Q + 1024;
constexpr int L_LAM = L_ST + 256;
constexpr int P_STR = 1040;
constexpr int L_P = L_LAM + 512;
constexpr int G_STR = 3600;
constexpr int LDS_BYTES = L_P + 32 * P_STR;

typedef __bf16 bf16x2_t __attribute__((ext_vector_type(2)));
__device__ __forceinline__ u32 cvtpk(float lo, float hi) { const bf16x2_t r = {(__bf16)lo, (__bf16)hi}; return __builtin_bit_cast(u32, r); }
__device__ __forceinline__ float u2f(u32 v) { return __builtin_bit_cast(float, v); }
__device__ __forceinline__ u32 f2u(float v) { return __builtin_bit_cast(u32, v); }
__device__ __forceinline__ float bflo(u32 v) { return __builtin_bit_cast(float, v << 16); }
__device__ __forceinline__ float bfhi(u32 v) { return __builtin_bit_cast(float, v & 0xffff0000u); }
template <int CTRL> __device__ __forceinline__ float dpp(float x) { return __builtin_bit_cast(float, __builtin_amdgcn_mov_dpp(__builtin_bit_cast(int, x), CTRL, 0xf, 0xf, true)); }
__device__ __forceinline__ float xrow_max(float x) {
  auto s = __builtin_amdgcn_permlane16_swap(f2u(x), f2u(x), false, false);
  x = fmaxf(u2f(s[0]), u2f(s[1]));
  auto t = __builtin_amdgcn_permlane32_swap(f2u(x), f2u(x), false, false);
  return fmaxf(u2f(t[0]), u2f(t[1]));
}
__device__ __forceinline__ float xrow_sum(float x) {
  auto s = __builtin_amdgcn_permlane16_swap(f2u(x), f2u(x), false, false);
  x = u2f(s[0]) + u2f(s[1]);
  auto t = __builtin_amdgcn_permlane32_swap(f2u(x), f2u(x), false, false);
  return u2f(t[0]) + u2f(t[1]);
}
__device__ __forceinline__ float wave_sum(float x) {
  x += dpp<0xB1>(x); x += dpp<0x4E>(x); x += dpp<0x141>(x); x += dpp<0x140>(x);
  return xrow_sum(x);
}
__device__ __forceinline__ f32x4 mfma32(u32x4 a, u32x4 b, f32x4 c) {
  return __builtin_amdgcn_mfma_f32_16x16x32_bf16(__builtin_bit_cast(bf16x8_t, a), __builtin_bit_cast(bf16x8_t, b), c, 0, 0, 0);
}
__device__ __forceinline__ f32x4 mfma16(u32x2 a, u32x2 b, f32x4 c) {
  return __builtin_amdgcn_mfma_f32_16x16x16bf16_1k(__builtin_bit_cast(s16x4, a), __builtin_bit_cast(s16x4, b), c, 0, 0, 0);
}
typedef short v4i16_t __attribute__((ext_vector_type(4)));
__device__ __forceinline__ u32x2 tr_read(LAS unsigned char* p) {
  return __builtin_bit_cast(u32x2, __builtin_amdgcn_ds_read_tr16_b64_v4i16((LAS v4i16_t*)p));
}
__device__ __forceinline__ u32x4 bfrag(LAS unsigned char* p, int second_off) {
  const u32x2 a = tr_read(p), b = tr_read(p + second_off);
  return u32x4{a.x, a.y, b.x, b.y};
}
__device__ __forceinline__ __amdgpu_buffer_rsrc_t mkrs(const void* p, u32 bytes) { return __builtin_amdgcn_make_buffer_rsrc((void*)p, 0, bytes, 0x00020000); }
template <int V> struct ic { static constexpr int value = V; };
#define LDB128(rs, vo, so, aux) __builtin_amdgcn_raw_buffer_load_b128(rs, vo, so, aux)
#define SB() __builtin_amdgcn_sched_barrier(0)
#define DRAIN() asm volatile("s_waitcnt vmcnt(0)" ::: "memory")
#define MEMFENCE() asm volatile("" ::: "memory")
#define RAWBAR() asm volatile("s_barrier" ::: "memory")
__device__ __forceinline__ void wg_barrier() { asm volatile("s_waitcnt lgkmcnt(0)\n\ts_barrier" ::: "memory"); }

__device__ __forceinline__ u32x4 ln_row(const float (&v)[8]) {
  float s = ((v[0] + v[1]) + (v[2] + v[3])) + ((v[4] + v[5]) + (v[6] + v[7]));
  const float mean = wave_sum(s) * (1.0f / DM);
  float d[8], q = 0.f;
#pragma unroll
  for (int i = 0; i < 8; ++i) { d[i] = v[i] - mean; q = fmaf(d[i], d[i], q); }
  const float var = wave_sum(q) * (1.0f / DM);
  const float rs = __builtin_amdgcn_rsqf(var + LN_EPS);
  return u32x4{cvtpk(d[0] * rs, d[1] * rs), cvtpk(d[2] * rs, d[3] * rs), cvtpk(d[4] * rs, d[5] * rs), cvtpk(d[6] * rs, d[7] * rs)};
}

#define STAMP(i) do {} while (0)
#define STAMP_FLUSH() do {} while (0)

__global__ void __launch_bounds__(NTHR, 2)
fused_decode_block(const u16* __restrict__ x, const u16* __restrict__ Kc, const u16* __restrict__ Vc, const u16* __restrict__ wq, const u16* __restrict__ wo,
                   const u16* __restrict__ wg, const u16* __restrict__ wu, const u16* __restrict__ wd, u16* __restrict__ out, unsigned char* __restrict__ ws) {
  __shared__ __attribute__((aligned(16))) unsigned char lds_raw[LDS_BYTES];
  LAS unsigned char* L = (LAS unsigned char*)lds_raw;
  const int tid = threadIdx.x, lane = tid & 63, w = __builtin_amdgcn_readfirstlane(tid >> 6);
  const int blk = blockIdx.x, kvh = blk & 7, j = blk >> 3, b = j >> 2, sp = j & 3;
  const int g = lane >> 4, c16 = lane & 15;
  GAS unsigned char* wsg = (GAS unsigned char*)ws;
  STAMP(0);

  u64 E;
  asm volatile("s_load_dwordx2 %0, %1, 0x0" : "=s"(E) : "s"(ws));
  const __amdgpu_buffer_rsrc_t rx = mkrs(x, NB * DM * 2);
  const __amdgpu_buffer_rsrc_t rwq = mkrs(wq, DM * HQ * DH * 2);
  const size_t kvbase = ((size_t)(b * HKV + kvh) * SK + (size_t)sp * TOK_WG) * DH;
  const __amdgpu_buffer_rsrc_t rk = mkrs(Kc + kvbase, TOK_WG * DH * 2);
  const __amdgpu_buffer_rsrc_t rv = mkrs(Vc + kvbase, TOK_WG * DH * 2);
  const __amdgpu_buffer_rsrc_t rws = mkrs(ws, WS_END);

  const int trl = (8 * g + ((lane >> 2) & 3)) * 32 + (lane & 3) * 8;
  const int jj = 3 * b + sp - 1;
  const int role = sp == 0 ? 1 : (jj < 7 ? 2 : (jj < 11 ? 3 : 0));
  const int cgu = kvh * 7 + jj, cdn = kvh * 4 + (jj - 7);
#define LOAD_WEIGHTS(wst) do { \
      const u16* p0 = role == 1 ? wo : (role == 2 ? wg : wd); \
      const u16* p1 = role == 2 ? wu : p0; \
      const u32 n0b = role == 0 ? 0u : (role == 1 ? (u32)(HQ * DH * DM * 2) : (u32)(DM * FF * 2)); \
      const u32 n1b = role == 3 ? 0u : n0b; \
      const __amdgpu_buffer_rsrc_t r0 = mkrs(p0, n0b), r1 = mkrs(p1, n0b), r2 = mkrs(p1, n1b); \
      const int v0o = role == 1 ? ((kvh * 512 + 128 * w + (lane >> 3)) * DM + 64 * b + 8 * (lane & 7)) * 2 \
                    : role == 2 ? ((128 * w + (lane >> 2)) * FF + 32 * cgu + 8 * (lane & 3)) * 2 \
                                : ((448 * w + (lane >> 1)) * DM + 16 * cdn + 8 * (lane & 1)) * 2; \
      const int st = role == 1 ? 8 * DM * 2 : role == 2 ? 16 * FF * 2 : 32 * DM * 2; \
      const int v8o = role == 2 ? v0o : v0o + 8 * st; \
      _Pragma("unroll") for (int i = 0; i < 8; ++i) wst[i] = LDB128(r0, v0o + i * st, 0, WAUX); \
      _Pragma("unroll") for (int i = 0; i < 6; ++i) wst[8 + i] = LDB128(r1, v8o + i * st, 0, WAUX); \
      _Pragma("unroll") for (int i = 6; i < 8; ++i) wst[8 + i] = LDB128(r2, v8o + i * st, 0, WAUX); \
      SB(); } while (0)
#define STAGE_WEIGHTS(wst) do { \
    if (role == 1) { \
      _Pragma("unroll") for (int i = 0; i < 16; ++i) { const int r = 128 * w + 8 * i + (lane >> 3), ch = lane & 7; \
        *(LAS u32x4*)(L + L_ROLE + (r ^ ((r >> 2) & 1)) * 128 + 32 * ((ch >> 1) ^ (((r >> 3) & 1) | (((r >> 5) & 1) << 1))) + 16 * (ch & 1)) = wst[i]; } \
    } else if (role == 2) { \
      _Pragma("unroll") for (int i = 0; i < 16; ++i) { const int r = 128 * w + 16 * (i & 7) + (lane >> 2), ch = lane & 3; \
        *(LAS u32x4*)(L + L_ROLE + (i >> 3) * 32768 + r * 64 + 32 * ((ch >> 1) ^ ((r >> 3) & 1)) + 16 * (ch & 1)) = wst[i]; } \
    } else if (role == 3) { \
      _Pragma("unroll") for (int i = 0; i < 14; ++i) *(LAS u32x4*)(L + L_ROLE + (448 * w + 32 * i) * 32 + lane * 16) = wst[i]; \
    } } while (0)
  u64 T64;
  if (w < 4) {
    const u32x4 xr0 = LDB128(rx, (2 * w * DM + 8 * lane) * 2, 0, 0), xr1 = LDB128(rx, ((2 * w + 1) * DM + 8 * lane) * 2, 0, 0);
    u32x4 wqr[4];
#pragma unroll
    for (int i = 0; i < 4; ++i) wqr[i] = LDB128(rwq, ((128 * sp + 32 * w + 8 * i + (lane >> 3)) * (HQ * DH) + kvh * 512 + 64 * b + 8 * (lane & 7)) * 2, 0, WAUX);
    SB(); RAWBAR(); SB();
    float v0[8], v1[8];
#pragma unroll
    for (int i = 0; i < 4; ++i) { v0[2 * i] = bflo(xr0[i]); v0[2 * i + 1] = bfhi(xr0[i]); v1[2 * i] = bflo(xr1[i]); v1[2 * i + 1] = bfhi(xr1[i]); }
    const u32x4 n0 = ln_row(v0), n1 = ln_row(v1);
    *(LAS u32x4*)(L + L_A + 2 * w * A_STR + lane * 16) = n0;
    *(LAS u32x4*)(L + L_A + (2 * w + 1) * A_STR + lane * 16) = n1;
#pragma unroll
    for (int i = 0; i < 4; ++i) *(LAS u32x4*)(L + L_WQ + (32 * w + 8 * i + (lane >> 3)) * 144 + (lane & 7) * 16) = wqr[i];
    STAMP(12);
    wg_barrier();
    STAMP(13);
    const u32x4 a = *(LAS u32x4*)(L + L_A + (lane & 7) * A_STR + (128 * sp + 32 * w + 8 * g) * 2);
#pragma unroll
    for (int nt = 0; nt < 4; ++nt) {
      LAS unsigned char* p = L + L_WQ + (32 * w + 8 * g + ((lane >> 2) & 3)) * 144 + nt * 32 + (lane & 3) * 8;
      const u32x2 lo = tr_read(p), hi = tr_read(p + 4 * 144);
      const f32x4 acc = mfma32(a, u32x4{lo.x, lo.y, hi.x, hi.y}, f32x4{0.f, 0.f, 0.f, 0.f});
      if (lane < 32) {
#pragma unroll
        for (int r = 0; r < 4; ++r) *(LAS float*)(L + L_P + ((w * 8 + 4 * g + r) * 64 + 16 * nt + c16) * 4) = acc[r];
      }
    }
    wg_barrier();
    STAMP(14);
    asm volatile("s_waitcnt lgkmcnt(0)" : "+s"(E));
    T64 = E + 1;
    const u64 T40 = T64 & 0xFFFFFFFFFFull;
#pragma unroll
    for (int i = 0; i < 2; ++i) {
      const int o = tid + 256 * i;
      float sum = 0.f;
#pragma unroll
      for (int ww = 0; ww < 4; ++ww) sum += *(LAS float*)(L + L_P + (ww * 512 + o) * 4);
      const u32 sv = f2u(sum) + 0x80u;
      __hip_atomic_store((GAS u64*)(wsg + WS_QG) + ((kvh * 8 + (o >> 6)) * 4 + sp) * 512 + 64 * b + (o & 63), (T40 << 24) | (u64)(sv >> 8), RLX);
    }
    STAMP(1);
    DRAIN();
    if (QSLEEP) __builtin_amdgcn_s_sleep(QSLEEP);
    STAMP(10);
    {
      GAS u64* qgp = (GAS u64*)(wsg + WS_QG) + (kvh * 8 + b) * 4 * 512 + tid;
      u64 qv[8]; int spins = 0;
      for (;;) {
        bool ok = true;
#pragma unroll
        for (int i = 0; i < 8; ++i) qv[i] = __hip_atomic_load(qgp + (i & 3) * 512 + (i >> 2) * 256, RLX);
#pragma unroll
        for (int i = 0; i < 8; ++i) ok &= (qv[i] >> 24) == T40;
        if (__all(ok) || ++spins > SPIN_MAX) break;
        MEMFENCE(); __builtin_amdgcn_s_sleep(2);
      }
      const float qa = (u2f((u32)qv[0] << 8) + u2f((u32)qv[1] << 8)) + (u2f((u32)qv[2] << 8) + u2f((u32)qv[3] << 8));
      const float qb = (u2f((u32)qv[4] << 8) + u2f((u32)qv[5] << 8)) + (u2f((u32)qv[6] << 8) + u2f((u32)qv[7] << 8));
      *(LAS u16*)(L + L_Q + tid * 2) = (u16)cvtpk(qa, qa);
      *(LAS u16*)(L + L_Q + 512 + tid * 2) = (u16)cvtpk(qb, qb);
    }
    STAMP(15);
    wg_barrier();
    SB();
    if (role == 1 || !LATEW) { u32x4 wst[16]; LOAD_WEIGHTS(wst); STAGE_WEIGHTS(wst); }
    wg_barrier();
  } else {
    const int sw = w - 4;
    const int koff = (4 * (c16 & 3) + (c16 >> 2)) * 256 + g * 16, voff = lane * 16;
    const bool vkeep = b >= VB;
#define ISSUE_K(slot, it) do { const int so_ = ((it) * 4 + sw) * 4096; \
    _Pragma("unroll") for (int u = 0; u < 4; ++u) Kb[slot][u] = LDB128(rk, koff + u * 64, so_, AUX_NT); SB(); } while (0)
#define ISSUE_V(slot, it) do { const int so_ = ((it) * 4 + sw) * 4096; \
    _Pragma("unroll") for (int u = 0; u < 4; ++u) Vb[slot][u] = LDB128(rv, voff + u * 1024, so_, VAUX); SB(); } while (0)
#define ISSUE(slot, it) do { ISSUE_K(slot, it); ISSUE_V(slot, it); } while (0)
    float mrun = -INFINITY, lrun = 0.f;
    f32x4 O1 = {0.f, 0.f, 0.f, 0.f}, O2 = {0.f, 0.f, 0.f, 0.f};
    auto stream = [&](auto vaux) __attribute__((always_inline)) {
    constexpr int VAUX = decltype(vaux)::value;
    u32x4 Kb[3][4], Vb[3][4];
    RAWBAR();
    u32 tw[2 * TLBN];
#pragma unroll
    for (int i = 0; i < TLBN; ++i) {
      const int o = ((i * 4 + sw) * 64 + lane) * TLBSTEP;
      tw[2 * i] = __builtin_amdgcn_raw_buffer_load_b32(rk, o, 0, AUX_NT); tw[2 * i + 1] = __builtin_amdgcn_raw_buffer_load_b32(rv, o, 0, AUX_NT);
    }
    SB();
    __builtin_amdgcn_s_sleep(127); __builtin_amdgcn_s_sleep(127); __builtin_amdgcn_s_sleep(127);
    if (SLEEP0) __builtin_amdgcn_s_sleep(SLEEP0);
    SB();
    ISSUE(0, 0);
    RAWBAR(); SB();
    ISSUE_V(1, 1);
    RAWBAR(); SB();
    ISSUE_K(1, 1);
    asm volatile("s_waitcnt vmcnt(4)" ::: "memory"); SB();
    ISSUE_K(2, 2);
    asm volatile("s_waitcnt vmcnt(4)" ::: "memory"); SB();
    ISSUE_V(2, 2);
#pragma unroll
    for (int i = 0; i < 2 * TLBN; ++i) asm volatile("" :: "v"(tw[i]));
    wg_barrier();
    STAMP(2);
    u32x4 Qf[4];
#pragma unroll
    for (int u = 0; u < 4; ++u) Qf[u] = *(LAS u32x4*)(L + L_Q + ((lane & 3) * 128 + 32 * u + 8 * g) * 2);
    const int jx = c16 >> 2;
    const u32 m0k = jx == 0 ? 0x0000FFFFu : jx == 1 ? 0xFFFF0000u : 0u;
    const u32 m1k = jx == 2 ? 0x0000FFFFu : jx == 3 ? 0xFFFF0000u : 0u;
#define PROCESS(slot) do { \
    f32x4 s_ = {0.f, 0.f, 0.f, 0.f}; \
    _Pragma("unroll") for (int u = 0; u < 4; ++u) s_ = mfma32(Kb[slot][u], Qf[u], s_); \
    const float tm_ = xrow_max(fmaxf(fmaxf(s_[0], s_[1]), fmaxf(s_[2], s_[3]))); \
    if (__any(tm_ > mrun)) { \
      const float mn_ = fmaxf(mrun, tm_), al_ = __builtin_amdgcn_exp2f((mrun - mn_) * SC_LOG2); \
      lrun *= al_; mrun = mn_; \
      const float a0_ = dpp<0x00>(al_), a1_ = dpp<0x55>(al_), a2_ = dpp<0xAA>(al_), a3_ = dpp<0xFF>(al_); \
      O1[0] *= a0_; O1[1] *= a1_; O1[2] *= a2_; O1[3] *= a3_; O2[0] *= a0_; O2[1] *= a1_; O2[2] *= a2_; O2[3] *= a3_; \
    } \
    const float mc_ = mrun * SC_LOG2; \
    _Pragma("unroll") for (int i = 0; i < 4; ++i) { \
      const float p_ = __builtin_amdgcn_exp2f(fmaf(s_[i], SC_LOG2, -mc_)); lrun += p_; \
      const u32 pv_ = cvtpk(p_, p_); const u32x2 a_ = {pv_ & m0k, pv_ & m1k}; \
      O1 = mfma16(a_, u32x2{Vb[slot][i].x, Vb[slot][i].y}, O1); O2 = mfma16(a_, u32x2{Vb[slot][i].z, Vb[slot][i].w}, O2); \
    } SB(); } while (0)
#pragma unroll 1
    for (int it = 0; it < 27; it += 3) { PROCESS(0); ISSUE(0, it + 3); PROCESS(1); ISSUE(1, it + 4); PROCESS(2); ISSUE(2, it + 5); }
    PROCESS(0); ISSUE(0, 30); PROCESS(1); ISSUE(1, 31); PROCESS(2); PROCESS(0); PROCESS(1);
    };
    if (vkeep) stream(ic<0>{}); else stream(ic<AUX_NT>{});
    asm volatile("s_waitcnt lgkmcnt(0)" : "+s"(E));
    T64 = E + 1;
    STAMP(3);
    lrun = xrow_sum(lrun);
#pragma unroll
    for (int h = 0; h < 4; ++h) *(LAS f32x2*)(L + L_MRG + ((sw * 4 + h) * 64 + lane) * 8) = f32x2{O1[h], O2[h]};
    if (lane < 4) { *(LAS float*)(L + L_ST + (lane * 4 + sw) * 4) = mrun; *(LAS float*)(L + L_ST + 64 + (lane * 4 + sw) * 4) = lrun; }
    wg_barrier();
  }
  const u32 T32 = (u32)T64;

  if (w == 0) {
    const int h = g;
    const f32x4 ma = *(LAS f32x4*)(L + L_ST + h * 16), la = *(LAS f32x4*)(L + L_ST + 64 + h * 16);
    const float M = fmaxf(fmaxf(ma[0], ma[1]), fmaxf(ma[2], ma[3]));
    float Ls = 0.f;
    f32x4 oa = {0.f, 0.f, 0.f, 0.f}, ob = {0.f, 0.f, 0.f, 0.f};
#pragma unroll
    for (int ww = 0; ww < 4; ++ww) {
      const float f = __builtin_amdgcn_exp2f((ma[ww] - M) * SC_LOG2);
      Ls = fmaf(f, la[ww], Ls);
      oa += f * *(LAS f32x4*)(L + L_MRG + ((ww * 4 + h) * 64 + 4 * c16) * 8);
      ob += f * *(LAS f32x4*)(L + L_MRG + ((ww * 4 + h) * 64 + 4 * c16) * 8 + 16);
    }
    const float inv = __builtin_amdgcn_rcpf(Ls);
    const u32x4 pk = {cvtpk(oa[0] * inv, oa[1] * inv), cvtpk(oa[2] * inv, oa[3] * inv), cvtpk(ob[0] * inv, ob[1] * inv), cvtpk(ob[2] * inv, ob[3] * inv)};
    __builtin_amdgcn_raw_buffer_store_b128(pk, rws, WS_PART + (kvh * 32 + j) * 1024 + lane * 16, 0, AUX_SC1);
    const float lam = fmaf(M, SC_LOG2, __builtin_amdgcn_logf(Ls));
    DRAIN();
    if (c16 == 0) __hip_atomic_store((GAS u64*)(wsg + WS_H1F + (kvh * 32 + j) * FS) + h, ((u64)T32 << 32) | f2u(lam), RLX);
  }
  STAMP(4);
  if (LATEW && role >= 2) {
    SB(); RAWBAR(); SB();
    if (w < 4) { u32x4 wst[16]; LOAD_WEIGHTS(wst); STAGE_WEIGHTS(wst); }
    wg_barrier();
  }

  const int rb = lane >> 3, cp = lane & 7;
  if (role == 1) {
    const int h = w >> 1, np = w & 1;
    u32x4 Bf[2][4];
    {
      const int q = (lane >> 2) & 3;
#pragma unroll
      for (int n = 0; n < 2; ++n)
#pragma unroll
        for (int u = 0; u < 4; ++u) {
          const int d0 = h * 128 + 32 * g + 8 * (q >> 1) + 4 * (q & 1) + u, d1 = d0 + 16;
          const int sg = (q >> 1) | ((g & 1) << 1);
          const u32x2 lo = tr_read(L + L_ROLE + (d0 ^ (q & 1)) * 128 + 32 * ((2 * np + n) ^ sg) + (lane & 3) * 8);
          const u32x2 hi = tr_read(L + L_ROLE + (d1 ^ (q & 1)) * 128 + 32 * ((2 * np + n) ^ sg) + (lane & 3) * 8);
          Bf[n][u] = u32x4{lo.x, lo.y, hi.x, hi.y};
        }
    }
    GAS u64* fp = (GAS u64*)(wsg + WS_H1F + (kvh * 32 + 4 * w + ((lane >> 2) & 3)) * FS) + (lane & 3);
    u64 f0; int spins = 0;
    for (;;) {
      f0 = __hip_atomic_load(fp, RLX);
      if (__all((u32)(f0 >> 32) == T32) || ++spins > SPIN_MAX) break;
      MEMFENCE(); __builtin_amdgcn_s_sleep(2);
    }
    MEMFENCE();
    u32x4 pv[4];
#pragma unroll
    for (int i = 0; i < 4; ++i) pv[i] = LDB128(rws, WS_PART + (kvh * 32 + 4 * w + i) * 1024 + lane * 16, 0, AUX_SC1);
    if (lane < 16) *(LAS u32*)(L + L_LAM + ((lane & 3) * 32 + 4 * w + (lane >> 2)) * 4) = (u32)f0;
#pragma unroll
    for (int i = 0; i < 4; ++i) *(LAS u32x4*)(L + L_P + (4 * w + i) * P_STR + lane * 16) = pv[i];
    wg_barrier();
#pragma unroll
    for (int T = 0; T < 2; ++T) {
      const f32x4 lm4 = *(LAS f32x4*)(L + L_LAM + (h * 32 + 16 * T + 4 * g) * 4);
      f32x4 a0 = {0.f, 0.f, 0.f, 0.f}, a1 = {0.f, 0.f, 0.f, 0.f};
#pragma unroll
      for (int u = 0; u < 4; ++u) {
        const u32x4 av = *(LAS u32x4*)(L + L_P + (16 * T + c16) * P_STR + h * 256 + u * 64 + g * 16);
        a0 = mfma32(av, Bf[0][u], a0); a1 = mfma32(av, Bf[1][u], a1);
      }
      const float lm = fmaxf(fmaxf(lm4[0], lm4[1]), fmaxf(lm4[2], lm4[3]));
      float e0 = __builtin_amdgcn_exp2f(lm4[0] - lm), e1 = __builtin_amdgcn_exp2f(lm4[1] - lm), e2 = __builtin_amdgcn_exp2f(lm4[2] - lm), e3 = __builtin_amdgcn_exp2f(lm4[3] - lm);
      const float inv = __builtin_amdgcn_rcpf((e0 + e1) + (e2 + e3));
      e0 *= inv; e1 *= inv; e2 *= inv; e3 *= inv;
      *(LAS float*)(L + L_RED + ((h * 8 + 4 * T + g) * 64 + 32 * np + c16) * 4) = (e0 * a0[0] + e1 * a0[1]) + (e2 * a0[2] + e3 * a0[3]);
      *(LAS float*)(L + L_RED + ((h * 8 + 4 * T + g) * 64 + 32 * np + 16 + c16) * 4) = (e0 * a1[0] + e1 * a1[1]) + (e2 * a1[2] + e3 * a1[3]);
    }
    wg_barrier();
    STAMP(5);
    if (w == 0) {
      f32x4 sa = {0.f, 0.f, 0.f, 0.f}, sb = {0.f, 0.f, 0.f, 0.f};
#pragma unroll
      for (int hh = 0; hh < 4; ++hh) { sa += *(LAS f32x4*)(L + L_RED + ((hh * 8 + rb) * 64 + 8 * cp) * 4); sb += *(LAS f32x4*)(L + L_RED + ((hh * 8 + rb) * 64 + 8 * cp) * 4 + 16); }
      const u32x4 pk = {cvtpk(sa[0], sa[1]), cvtpk(sa[2], sa[3]), cvtpk(sb[0], sb[1]), cvtpk(sb[2], sb[3])};
      __builtin_amdgcn_raw_buffer_store_b128(pk, rws, WS_Y + ((kvh * 8 + rb) * DM + 64 * b + 8 * cp) * 2, 0, AUX_SC1);
      DRAIN();
      if (lane == 0) __hip_atomic_store((GAS u64*)(wsg + WS_H2F + (kvh * 8 + b) * FS), T64, RLX);
    }
    STAMP(6);
  } else if (role == 2) {
    const int mat = w & 1, kq = w >> 1;
    u32x4 Bf[2][4];
    {
      const int q = (lane >> 2) & 3;
#pragma unroll
      for (int n = 0; n < 2; ++n)
#pragma unroll
        for (int u = 0; u < 4; ++u) {
          LAS unsigned char* p = L + L_ROLE + mat * 32768 + (128 * kq + 32 * u + 8 * g + q) * 64 + 32 * (n ^ (g & 1)) + (lane & 3) * 8;
          const u32x2 lo = tr_read(p), hi = tr_read(p + 256);
          Bf[n][u] = u32x4{lo.x, lo.y, hi.x, hi.y};
        }
    }
    const u32x4 xv = LDB128(rx, (w * DM + 8 * lane) * 2, 0, 0);
    if (w == 0) {
      GAS u64* fp = (GAS u64*)(wsg + WS_H2F + lane * FS); int spins = 0;
      for (;;) {
        const u64 f0 = __hip_atomic_load(fp, RLX);
        if (__all(f0 == T64) || ++spins > SPIN_MAX) break;
        MEMFENCE(); __builtin_amdgcn_s_sleep(1);
      }
    }
    wg_barrier();
    STAMP(7);
    u32x4 yv[8];
#pragma unroll
    for (int i = 0; i < 8; ++i) yv[i] = LDB128(rws, WS_Y + ((i * 8 + w) * DM + 8 * lane) * 2, 0, AUX_SC1);
    float v[8];
#pragma unroll
    for (int i = 0; i < 4; ++i) { v[2 * i] = bflo(xv[i]); v[2 * i + 1] = bfhi(xv[i]); }
#pragma unroll
    for (int i = 0; i < 8; ++i) {
#pragma unroll
      for (int e = 0; e < 4; ++e) { v[2 * e] += bflo(yv[i][e]); v[2 * e + 1] += bfhi(yv[i][e]); }
    }
    *(LAS u32x4*)(L + L_A + w * A_STR + lane * 16) = ln_row(v);
    wg_barrier();
    STAMP(8);
    f32x4 a0 = {0.f, 0.f, 0.f, 0.f}, a1 = {0.f, 0.f, 0.f, 0.f};
#pragma unroll
    for (int u = 0; u < 4; ++u) {
      const u32x4 av = *(LAS u32x4*)(L + L_A + (lane & 7) * A_STR + (128 * kq + 32 * u + 8 * g) * 2);
      a0 = mfma32(av, Bf[0][u], a0); a1 = mfma32(av, Bf[1][u], a1);
    }
    if (lane < 32) {
#pragma unroll
      for (int r = 0; r < 4; ++r) {
        *(LAS float*)(L + L_RED + (((kq * 2 + mat) * 8 + 4 * g + r) * 32 + c16) * 4) = a0[r];
        *(LAS float*)(L + L_RED + (((kq * 2 + mat) * 8 + 4 * g + r) * 32 + 16 + c16) * 4) = a1[r];
      }
    }
    wg_barrier();
    if (w == 0) {
      f32x4 ga = {0.f, 0.f, 0.f, 0.f}, up = {0.f, 0.f, 0.f, 0.f};
#pragma unroll
      for (int q = 0; q < 4; ++q) { ga += *(LAS f32x4*)(L + L_RED + (((q * 2 + 0) * 8 + rb) * 32 + 4 * cp) * 4); up += *(LAS f32x4*)(L + L_RED + (((q * 2 + 1) * 8 + rb) * 32 + 4 * cp) * 4); }
      float r[4];
#pragma unroll
      for (int e = 0; e < 4; ++e) {
        const float z = ga[e];
        const float t = z * fmaf(z * z, 0.044715f * 0.7978845608028654f, 0.7978845608028654f);
        const float sg = __builtin_amdgcn_rcpf(1.0f + __builtin_amdgcn_exp2f(t * (-2.0f * 1.4426950408889634f)));
        r[e] = z * sg * up[e];
      }
      __builtin_amdgcn_raw_buffer_store_b64(u32x2{cvtpk(r[0], r[1]), cvtpk(r[2], r[3])}, rws, WS_G + (rb * FF + 32 * cgu + 4 * cp) * 2, 0, AUX_SC1);
      DRAIN();
      if (lane == 0) __hip_atomic_store((GAS u64*)(wsg + WS_H3F + cgu * FS), T64, RLX);
    }
    STAMP(9);
  } else if (role == 3) {
    u32x4 Bf[7];
#pragma unroll
    for (int u = 0; u < 7; ++u) Bf[u] = bfrag(L + L_ROLE + (224 * w + 32 * u) * 32 + trl, 128);
    f32x2 hres = {0.f, 0.f};
    if (w == 0) {
      const u32 xw = *(const GAS u32*)((const GAS unsigned char*)x + (rb * DM + 16 * cdn + 2 * cp) * 2);
      GAS u64* fp = (GAS u64*)(wsg + WS_H2F + ((lane & 7) * 8 + (cdn >> 2)) * FS); int spins = 0;
      for (;;) { const u64 f0 = __hip_atomic_load(fp, RLX); if (__all(f0 == T64) || ++spins > SPIN_MAX) break; MEMFENCE(); __builtin_amdgcn_s_sleep(2); }
      MEMFENCE();
      u32 yy[8];
#pragma unroll
      for (int q = 0; q < 8; ++q) yy[q] = __builtin_amdgcn_raw_buffer_load_b32(rws, WS_Y + ((q * 8 + rb) * DM + 16 * cdn + 2 * cp) * 2, 0, AUX_SC1);
      hres = f32x2{bflo(xw), bfhi(xw)};
#pragma unroll
      for (int q = 0; q < 8; ++q) hres += f32x2{bflo(yy[q]), bfhi(yy[q])};
      GAS u64* gp = (GAS u64*)(wsg + WS_H3F + lane * FS); spins = 0;
      for (;;) {
        const u64 f0 = lane < NGU ? __hip_atomic_load(gp, RLX) : T64;
        if (__all(f0 == T64) || ++spins > SPIN_MAX) break;
        MEMFENCE(); __builtin_amdgcn_s_sleep(1);
      }
      if (cdn == 0 && lane == 0) __hip_atomic_store((GAS u64*)(wsg + WS_E), T64, RLX);
    }
    wg_barrier();
    STAMP(9);
    {
      u32x4 gv[4];
#pragma unroll
      for (int i = 0; i < 4; ++i) gv[i] = LDB128(rws, (i == 3 && w >= 4) ? WS_END : WS_G + ((w + 8 * i) * 64 + lane) * 16, 0, AUX_SC1);
#pragma unroll
      for (int i = 0; i < 4; ++i) { const int ci = (w + 8 * i) * 64 + lane, row = ci / 224; if (i < 3 || w < 4) *(LAS u32x4*)(L + L_P + row * G_STR + (ci - row * 224) * 16) = gv[i]; }
    }
    wg_barrier();
    f32x4 acc = {0.f, 0.f, 0.f, 0.f};
#pragma unroll
    for (int u = 0; u < 7; ++u) acc = mfma32(*(LAS u32x4*)(L + L_P + (lane & 7) * G_STR + ((7 * w + u) * 32 + 8 * g) * 2), Bf[u], acc);
    if (lane < 32) {
#pragma unroll
      for (int r = 0; r < 4; ++r) *(LAS float*)(L + L_RED + ((w * 8 + 4 * g + r) * 16 + c16) * 4) = acc[r];
    }
    wg_barrier();
    if (w == 0) {
      f32x2 s = hres;
#pragma unroll
      for (int ww = 0; ww < 8; ++ww) s += *(LAS f32x2*)(L + L_RED + ((ww * 8 + rb) * 16 + 2 * cp) * 4);
      *(GAS u32*)((GAS unsigned char*)out + (rb * DM + 16 * cdn + 2 * cp) * 2) = cvtpk(s[0], s[1]);
    }
    STAMP(8);
  }
  STAMP_FLUSH();
}

extern "C" void kernel_launch(void* const* d_in, const int* in_sizes, int n_in, void* d_out, int out_size, void* d_ws, size_t ws_size, hipStream_t stream) {
  fused_decode_block<<<NWG, NTHR, 0, stream>>>((const u16*)d_in[0], (const u16*)d_in[1], (const u16*)d_in[2], (const u16*)d_in[3], (const u16*)d_in[4],
                                              (const u16*)d_in[6], (const u16*)d_in[7], (const u16*)d_in[8], (u16*)d_out, (unsigned char*)d_ws);
}
```

```cpp
#include <hip/hip_runtime.h>
#include <hip/hip_bf16.h>
#include <stdint.h>

typedef unsigned short u16;
typedef unsigned u32;
typedef unsigned long long u64;
typedef u32 u32x4 __attribute__((ext_vector_type(4)));
typedef u32 u32x2 __attribute__((ext_vector_type(2)));
typedef float f32x4 __attribute__((ext_vector_type(4)));
typedef float f32x2 __attribute__((ext_vector_type(2)));
typedef short s16x8 __attribute__((ext_vector_type(8)));
typedef short s16x4 __attribute__((ext_vector_type(4)));
typedef __bf16 bf16x8_t __attribute__((ext_vector_type(8)));
#define GAS __attribute__((address_space(1)))
#define LAS __attribute__((address_space(3)))
#define RLX __ATOMIC_RELAXED, __HIP_MEMORY_SCOPE_AGENT

#define QPOS 0
#define HOLD 1
#define PASSES 1
#define SLEEP0 8
#define QSLEEP 4
#define LATEW 1
#define VB 8
#define KB 8
#define WAUX 2
#define STAMPS 0

constexpr int NB = 8, DM = 512, HQ = 32, HKV = 8, SK = 8192, DH = 128, FF = 1792;
constexpr int NWG = 256, NTHR = 512;
constexpr int NGU = FF / 32;
constexpr int TOK_WG = SK / 4;
constexpr int NIT = TOK_WG / 16 / 8;
constexpr float SC_LOG2 = 0.08838834764831845f * 1.4426950408889634f;
constexpr float LN_EPS = 1e-5f;
constexpr int AUX_NT = 2, AUX_SC1 = 16;
constexpr int SPIN_MAX = 1 << 16;

constexpr int FS = 256;
constexpr int WS_E    = 0;
constexpr int WS_H1F  = 4096;
constexpr int WS_H2F  = WS_H1F + 256 * FS;
constexpr int WS_H3F  = WS_H2F + 64 * FS;
constexpr int WS_QG   = WS_H3F + 64 * FS;
constexpr int WS_PART = WS_QG + HKV * NB * 4 * 512 * 8;
constexpr int WS_Y    = WS_PART + HKV * 32 * 4 * 128 * 2;
constexpr int WS_G    = WS_Y + HKV * NB * DM * 2;
constexpr int WS_END  = WS_G + NB * FF * 2;
constexpr int WS_STAMP = 2 << 20;

constexpr int L_ROLE = 0;
constexpr int L_WQ = 0;
constexpr int L_MRG = 65536;
constexpr int A_STR = 1040;
constexpr int L_A = L_MRG + 16384;
constexpr int L_RED = L_A + 8 * A_STR;
constexpr int L_Q = L_RED + 8192;
constexpr int L_ST = L_Q + 1024;
constexpr int L_LAM = L_ST + 256;
constexpr int P_STR = 1040;
constexpr int L_P = L_LAM + 512;
constexpr int G_STR = 3600;
constexpr int LDS_BYTES = L_P + 32 * P_STR;

typedef __bf16 bf16x2_t __attribute__((ext_vector_type(2)));
__device__ __forceinline__ u32 cvtpk(float lo, float hi) { const bf16x2_t r = {(__bf16)lo, (__bf16)hi}; return __builtin_bit_cast(u32, r); }
__device__ __forceinline__ float u2f(u32 v) { return __builtin_bit_cast(float, v); }
__device__ __forceinline__ u32 f2u(float v) { return __builtin_bit_cast(u32, v); }
__device__ __forceinline__ float bflo(u32 v) { return __builtin_bit_cast(float, v << 16); }
__device__ __forceinline__ float bfhi(u32 v) { return __builtin_bit_cast(float, v & 0xffff0000u); }
template <int CTRL> __device__ __forceinline__ float dpp(float x) { return __builtin_bit_cast(float, __builtin_amdgcn_mov_dpp(__builtin_bit_cast(int, x), CTRL, 0xf, 0xf, true)); }
__device__ __forceinline__ float xrow_max(float x) {
  auto s = __builtin_amdgcn_permlane16_swap(f2u(x), f2u(x), false, false);
  x = fmaxf(u2f(s[0]), u2f(s[1]));
  auto t = __builtin_amdgcn_permlane32_swap(f2u(x), f2u(x), false, false);
  return fmaxf(u2f(t[0]), u2f(t[1]));
}
__device__ __forceinline__ float xrow_sum(float x) {
  auto s = __builtin_amdgcn_permlane16_swap(f2u(x), f2u(x), false, false);
  x = u2f(s[0]) + u2f(s[1]);
  auto t = __builtin_amdgcn_permlane32_swap(f2u(x), f2u(x), false, false);
  return u2f(t[0]) + u2f(t[1]);
}
__device__ __forceinline__ float wave_sum(float x) {
  x += dpp<0xB1>(x); x += dpp<0x4E>(x); x += dpp<0x141>(x); x += dpp<0x140>(x);
  return xrow_sum(x);
}
__device__ __forceinline__ f32x4 mfma32(u32x4 a, u32x4 b, f32x4 c) {
  return __builtin_amdgcn_mfma_f32_16x16x32_bf16(__builtin_bit_cast(bf16x8_t, a), __builtin_bit_cast(bf16x8_t, b), c, 0, 0, 0);
}
__device__ __forceinline__ f32x4 mfma16(u32x2 a, u32x2 b, f32x4 c) {
  return __builtin_amdgcn_mfma_f32_16x16x16bf16_1k(__builtin_bit_cast(s16x4, a), __builtin_bit_cast(s16x4, b), c, 0, 0, 0);
}
typedef short v4i16_t __attribute__((ext_vector_type(4)));
__device__ __forceinline__ u32x2 tr_read(LAS unsigned char* p) {
  return __builtin_bit_cast(u32x2, __builtin_amdgcn_ds_read_tr16_b64_v4i16((LAS v4i16_t*)p));
}
__device__ __forceinline__ u32x4 bfrag(LAS unsigned char* p, int second_off) {
  const u32x2 a = tr_read(p), b = tr_read(p + second_off);
  return u32x4{a.x, a.y, b.x, b.y};
}
__device__ __forceinline__ __amdgpu_buffer_rsrc_t mkrs(const void* p, u32 bytes) { return __builtin_amdgcn_make_buffer_rsrc((void*)p, 0, bytes, 0x00020000); }
template <int V> struct ic { static constexpr int value = V; };
#define LDB128(rs, vo, so, aux) __builtin_amdgcn_raw_buffer_load_b128(rs, vo, so, aux)
#define SB() __builtin_amdgcn_sched_barrier(0)
#define DRAIN() asm volatile("s_waitcnt vmcnt(0)" ::: "memory")
#define MEMFENCE() asm volatile("" ::: "memory")
#define RAWBAR() asm volatile("s_barrier" ::: "memory")
__device__ __forceinline__ void wg_barrier() { asm volatile("s_waitcnt lgkmcnt(0)\n\ts_barrier" ::: "memory"); }

__device__ __forceinline__ u32x4 ln_row(const float (&v)[8]) {
  float s = ((v[0] + v[1]) + (v[2] + v[3])) + ((v[4] + v[5]) + (v[6] + v[7]));
  const float mean = wave_sum(s) * (1.0f / DM);
  float d[8], q = 0.f;
#pragma unroll
  for (int i = 0; i < 8; ++i) { d[i] = v[i] - mean; q = fmaf(d[i], d[i], q); }
  const float var = wave_sum(q) * (1.0f / DM);
  const float rs = __builtin_amdgcn_rsqf(var + LN_EPS);
  return u32x4{cvtpk(d[0] * rs, d[1] * rs), cvtpk(d[2] * rs, d[3] * rs), cvtpk(d[4] * rs, d[5] * rs), cvtpk(d[6] * rs, d[7] * rs)};
}

#define STAMP(i) do {} while (0)
#define STAMP_FLUSH() do {} while (0)

__global__ void __launch_bounds__(NTHR, 2)
fused_decode_block(const u16* __restrict__ x, const u16* __restrict__ Kc, const u16* __restrict__ Vc, const u16* __restrict__ wq, const u16* __restrict__ wo,
                   const u16* __restrict__ wg, const u16* __restrict__ wu, const u16* __restrict__ wd, u16* __restrict__ out, unsigned char* __restrict__ ws) {
  __shared__ __attribute__((aligned(16))) unsigned char lds_raw[LDS_BYTES];
  LAS unsigned char* L = (LAS unsigned char*)lds_raw;
  const int tid = threadIdx.x, lane = tid & 63, w = __builtin_amdgcn_readfirstlane(tid >> 6);
  const int blk = blockIdx.x, kvh = blk & 7, j = blk >> 3, b = j >> 2, sp = j & 3;
  const int g = lane >> 4, c16 = lane & 15;
  GAS unsigned char* wsg = (GAS unsigned char*)ws;
  STAMP(0);

  u64 E;
  asm volatile("s_load_dwordx2 %0, %1, 0x0" : "=s"(E) : "s"(ws));
  const __amdgpu_buffer_rsrc_t rx = mkrs(x, NB * DM * 2);
  const __amdgpu_buffer_rsrc_t rwq = mkrs(wq, DM * HQ * DH * 2);
  const size_t kvbase = ((size_t)(b * HKV + kvh) * SK + (size_t)sp * TOK_WG) * DH;
  const __amdgpu_buffer_rsrc_t rk = mkrs(Kc + kvbase, TOK_WG * DH * 2);
  const __amdgpu_buffer_rsrc_t rv = mkrs(Vc + kvbase, TOK_WG * DH * 2);
  const __amdgpu_buffer_rsrc_t rws = mkrs(ws, WS_END);

  const int trl = (8 * g + ((lane >> 2) & 3)) * 32 + (lane & 3) * 8;
  const int jj = 3 * b + sp - 1;
  const int role = sp == 0 ? 1 : (jj < 7 ? 2 : (jj < 11 ? 3 : 0));
  const int cgu = kvh * 7 + jj, cdn = kvh * 4 + (jj - 7);
#define LOAD_WEIGHTS(wst) do { \
      const u16* p0 = role == 1 ? wo : (role == 2 ? wg : wd); \
      const u16* p1 = role == 2 ? wu : p0; \
      const u32 n0b = role == 0 ? 0u : (role == 1 ? (u32)(HQ * DH * DM * 2) : (u32)(DM * FF * 2)); \
      const u32 n1b = role == 3 ? 0u : n0b; \
      const __amdgpu_buffer_rsrc_t r0 = mkrs(p0, n0b), r1 = mkrs(p1, n0b), r2 = mkrs(p1, n1b); \
      const int v0o = role == 1 ? ((kvh * 512 + 128 * w + (lane >> 3)) * DM + 64 * b + 8 * (lane & 7)) * 2 \
                    : role == 2 ? ((128 * w + (lane >> 2)) * FF + 32 * cgu + 8 * (lane & 3)) * 2 \
                                : ((448 * w + (lane >> 1)) * DM + 16 * cdn + 8 * (lane & 1)) * 2; \
      const int st = role == 1 ? 8 * DM * 2 : role == 2 ? 16 * FF * 2 : 32 * DM * 2; \
      const int v8o = role == 2 ? v0o : v0o + 8 * st; \
      _Pragma("unroll") for (int i = 0; i < 8; ++i) wst[i] = LDB128(r0, v0o + i * st, 0, WAUX); \
      _Pragma("unroll") for (int i = 0; i < 6; ++i) wst[8 + i] = LDB128(r1, v8o + i * st, 0, WAUX); \
      _Pragma("unroll") for (int i = 6; i < 8; ++i) wst[8 + i] = LDB128(r2, v8o + i * st, 0, WAUX); \
      SB(); } while (0)
#define STAGE_WEIGHTS(wst) do { \
    if (role == 1) { \
      _Pragma("unroll") for (int i = 0; i < 16; ++i) { const int r = 128 * w + 8 * i + (lane >> 3), ch = lane & 7; \
        *(LAS u32x4*)(L + L_ROLE + (r ^ ((r >> 2) & 1)) * 128 + 32 * ((ch >> 1) ^ (((r >> 3) & 1) | (((r >> 5) & 1) << 1))) + 16 * (ch & 1)) = wst[i]; } \
    } else if (role == 2) { \
      _Pragma("unroll") for (int i = 0; i < 16; ++i) { const int r = 128 * w + 16 * (i & 7) + (lane >> 2), ch = lane & 3; \
        *(LAS u32x4*)(L + L_ROLE + (i >> 3) * 32768 + r * 64 + 32 * ((ch >> 1) ^ ((r >> 3) & 1)) + 16 * (ch & 1)) = wst[i]; } \
    } else if (role == 3) { \
      _Pragma("unroll") for (int i = 0; i < 14; ++i) *(LAS u32x4*)(L + L_ROLE + (448 * w + 32 * i) * 32 + lane * 16) = wst[i]; \
    } } while (0)
  u64 T64;
  if (w < 4) {
    const u32x4 xr0 = LDB128(rx, (2 * w * DM + 8 * lane) * 2, 0, 0), xr1 = LDB128(rx, ((2 * w + 1) * DM + 8 * lane) * 2, 0, 0);
    u32x4 wqr[4];
#pragma unroll
    for (int i = 0; i < 4; ++i) wqr[i] = LDB128(rwq, ((128 * sp + 32 * w + 8 * i + (lane >> 3)) * (HQ * DH) + kvh * 512 + 64 * b + 8 * (lane & 7)) * 2, 0, WAUX);
    SB(); RAWBAR(); SB();
    float v0[8], v1[8];
#pragma unroll
    for (int i = 0; i < 4; ++i) { v0[2 * i] = bflo(xr0[i]); v0[2 * i + 1] = bfhi(xr0[i]); v1[2 * i] = bflo(xr1[i]); v1[2 * i + 1] = bfhi(xr1[i]); }
    const u32x4 n0 = ln_row(v0), n1 = ln_row(v1);
    *(LAS u32x4*)(L + L_A + 2 * w * A_STR + lane * 16) = n0;
    *(LAS u32x4*)(L + L_A + (2 * w + 1) * A_STR + lane * 16) = n1;
#pragma unroll
    for (int i = 0; i < 4; ++i) *(LAS u32x4*)(L + L_WQ + (32 * w + 8 * i + (lane >> 3)) * 144 + (lane & 7) * 16) = wqr[i];
    STAMP(12);
    wg_barrier();
    STAMP(13);
    const u32x4 a = *(LAS u32x4*)(L + L_A + (lane & 7) * A_STR + (128 * sp + 32 * w + 8 * g) * 2);
#pragma unroll
    for (int nt = 0; nt < 4; ++nt) {
      LAS unsigned char* p = L + L_WQ + (32 * w + 8 * g + ((lane >> 2) & 3)) * 144 + nt * 32 + (lane & 3) * 8;
      const u32x2 lo = tr_read(p), hi = tr_read(p + 4 * 144);
      const f32x4 acc = mfma32(a, u32x4{lo.x, lo.y, hi.x, hi.y}, f32x4{0.f, 0.f, 0.f, 0.f});
      if (lane < 32) {
#pragma unroll
        for (int r = 0; r < 4; ++r) *(LAS float*)(L + L_P + ((w * 8 + 4 * g + r) * 64 + 16 * nt + c16) * 4) = acc[r];
      }
    }
    wg_barrier();
    STAMP(14);
    asm volatile("s_waitcnt lgkmcnt(0)" : "+s"(E));
    T64 = E + 1;
    const u64 T40 = T64 & 0xFFFFFFFFFFull;
#pragma unroll
    for (int i = 0; i < 2; ++i) {
      const int o = tid + 256 * i;
      float sum = 0.f;
#pragma unroll
      for (int ww = 0; ww < 4; ++ww) sum += *(LAS float*)(L + L_P + (ww * 512 + o) * 4);
      const u32 sv = f2u(sum) + 0x80u;
      __hip_atomic_store((GAS u64*)(wsg + WS_QG) + ((kvh * 8 + (o >> 6)) * 4 + sp) * 512 + 64 * b + (o & 63), (T40 << 24) | (u64)(sv >> 8), RLX);
    }
    STAMP(1);
    DRAIN();
    if (QSLEEP) __builtin_amdgcn_s_sleep(QSLEEP);
    STAMP(10);
    {
      GAS u64* qgp = (GAS u64*)(wsg + WS_QG) + (kvh * 8 + b) * 4 * 512 + tid;
      u64 qv[8]; int spins = 0;
      for (;;) {
        bool ok = true;
#pragma unroll
        for (int i = 0; i < 8; ++i) qv[i] = __hip_atomic_load(qgp + (i & 3) * 512 + (i >> 2) * 256, RLX);
#pragma unroll
        for (int i = 0; i < 8; ++i) ok &= (qv[i] >> 24) == T40;
        if (__all(ok) || ++spins > SPIN_MAX) break;
        MEMFENCE(); __builtin_amdgcn_s_sleep(2);
      }
      const float qa = (u2f((u32)qv[0] << 8) + u2f((u32)qv[1] << 8)) + (u2f((u32)qv[2] << 8) + u2f((u32)qv[3] << 8));
      const float qb = (u2f((u32)qv[4] << 8) + u2f((u32)qv[5] << 8)) + (u2f((u32)qv[6] << 8) + u2f((u32)qv[7] << 8));
      *(LAS u16*)(L + L_Q + tid * 2) = (u16)cvtpk(qa, qa);
      *(LAS u16*)(L + L_Q + 512 + tid * 2) = (u16)cvtpk(qb, qb);
    }
    STAMP(15);
    wg_barrier();
    SB();
    if (role == 1 || !LATEW) { u32x4 wst[16]; LOAD_WEIGHTS(wst); STAGE_WEIGHTS(wst); }
    wg_barrier();
  } else {
    const int sw = w - 4;
    const int koff = (4 * (c16 & 3) + (c16 >> 2)) * 256 + g * 16, voff = lane * 16;
    const bool vkeep = b >= VB;
#define ISSUE_K(slot, it) do { const int so_ = ((it) * 4 + sw) * 4096; \
    _Pragma("unroll") for (int u = 0; u < 4; ++u) Kb[slot][u] = LDB128(rk, koff + u * 64, so_, KAUX); SB(); } while (0)
#define ISSUE_V(slot, it) do { const int so_ = ((it) * 4 + sw) * 4096; \
    _Pragma("unroll") for (int u = 0; u < 4; ++u) Vb[slot][u] = LDB128(rv, voff + u * 1024, so_, VAUX); SB(); } while (0)
#define ISSUE(slot, it) do { ISSUE_K(slot, it); ISSUE_V(slot, it); } while (0)
    float mrun = -INFINITY, lrun = 0.f;
    f32x4 O1 = {0.f, 0.f, 0.f, 0.f}, O2 = {0.f, 0.f, 0.f, 0.f};
    auto stream = [&](auto kaux, auto vaux) __attribute__((always_inline)) {
    constexpr int KAUX = decltype(kaux)::value, VAUX = decltype(vaux)::value;
    u32x4 Kb[3][4], Vb[3][4];
    RAWBAR();
    if (SLEEP0) __builtin_amdgcn_s_sleep(SLEEP0);
    SB();
    ISSUE(0, 0);
    RAWBAR(); SB();
    ISSUE_V(1, 1);
    RAWBAR(); SB();
    ISSUE_K(1, 1);
    asm volatile("s_waitcnt vmcnt(4)" ::: "memory"); SB();
    ISSUE_K(2, 2);
    asm volatile("s_waitcnt vmcnt(4)" ::: "memory"); SB();
    ISSUE_V(2, 2);
    wg_barrier();
    STAMP(2);
    u32x4 Qf[4];
#pragma unroll
    for (int u = 0; u < 4; ++u) Qf[u] = *(LAS u32x4*)(L + L_Q + ((lane & 3) * 128 + 32 * u + 8 * g) * 2);
    const int jx = c16 >> 2;
    const u32 m0k = jx == 0 ? 0x0000FFFFu : jx == 1 ? 0xFFFF0000u : 0u;
    const u32 m1k = jx == 2 ? 0x0000FFFFu : jx == 3 ? 0xFFFF0000u : 0u;
#define PROCESS(slot) do { \
    f32x4 s_ = {0.f, 0.f, 0.f, 0.f}; \
    _Pragma("unroll") for (int u = 0; u < 4; ++u) s_ = mfma32(Kb[slot][u], Qf[u], s_); \
    const float tm_ = xrow_max(fmaxf(fmaxf(s_[0], s_[1]), fmaxf(s_[2], s_[3]))); \
    if (__any(tm_ > mrun)) { \
      const float mn_ = fmaxf(mrun, tm_), al_ = __builtin_amdgcn_exp2f((mrun - mn_) * SC_LOG2); \
      lrun *= al_; mrun = mn_; \
      const float a0_ = dpp<0x00>(al_), a1_ = dpp<0x55>(al_), a2_ = dpp<0xAA>(al_), a3_ = dpp<0xFF>(al_); \
      O1[0] *= a0_; O1[1] *= a1_; O1[2] *= a2_; O1[3] *= a3_; O2[0] *= a0_; O2[1] *= a1_; O2[2] *= a2_; O2[3] *= a3_; \
    } \
    const float mc_ = mrun * SC_LOG2; \
    _Pragma("unroll") for (int i = 0; i < 4; ++i) { \
      const float p_ = __builtin_amdgcn_exp2f(fmaf(s_[i], SC_LOG2, -mc_)); lrun += p_; \
      const u32 pv_ = cvtpk(p_, p_); const u32x2 a_ = {pv_ & m0k, pv_ & m1k}; \
      O1 = mfma16(a_, u32x2{Vb[slot][i].x, Vb[slot][i].y}, O1); O2 = mfma16(a_, u32x2{Vb[slot][i].z, Vb[slot][i].w}, O2); \
    } SB(); } while (0)
#pragma unroll 1
    for (int it = 0; it < 27; it += 3) { PROCESS(0); ISSUE(0, it + 3); PROCESS(1); ISSUE(1, it + 4); PROCESS(2); ISSUE(2, it + 5); }
    PROCESS(0); ISSUE(0, 30); PROCESS(1); ISSUE(1, 31); PROCESS(2); PROCESS(0); PROCESS(1);
    };
    if (vkeep) stream(ic<0>{}, ic<0>{}); else stream(ic<AUX_NT>{}, ic<AUX_NT>{});
    asm volatile("s_waitcnt lgkmcnt(0)" : "+s"(E));
    T64 = E + 1;
    STAMP(3);
    lrun = xrow_sum(lrun);
#pragma unroll
    for (int h = 0; h < 4; ++h) *(LAS f32x2*)(L + L_MRG + ((sw * 4 + h) * 64 + lane) * 8) = f32x2{O1[h], O2[h]};
    if (lane < 4) { *(LAS float*)(L + L_ST + (lane * 4 + sw) * 4) = mrun; *(LAS float*)(L + L_ST + 64 + (lane * 4 + sw) * 4) = lrun; }
    wg_barrier();
  }
  const u32 T32 = (u32)T64;

  if (w == 0) {
    const int h = g;
    const f32x4 ma = *(LAS f32x4*)(L + L_ST + h * 16), la = *(LAS f32x4*)(L + L_ST + 64 + h * 16);
    const float M = fmaxf(fmaxf(ma[0], ma[1]), fmaxf(ma[2], ma[3]));
    float Ls = 0.f;
    f32x4 oa = {0.f, 0.f, 0.f, 0.f}, ob = {0.f, 0.f, 0.f, 0.f};
#pragma unroll
    for (int ww = 0; ww < 4; ++ww) {
      const float f = __builtin_amdgcn_exp2f((ma[ww] - M) * SC_LOG2);
      Ls = fmaf(f, la[ww], Ls);
      oa += f * *(LAS f32x4*)(L + L_MRG + ((ww * 4 + h) * 64 + 4 * c16) * 8);
      ob += f * *(LAS f32x4*)(L + L_MRG + ((ww * 4 + h) * 64 + 4 * c16) * 8 + 16);
    }
    const float inv = __builtin_amdgcn_rcpf(Ls);
    const u32x4 pk = {cvtpk(oa[0] * inv, oa[1] * inv), cvtpk(oa[2] * inv, oa[3] * inv), cvtpk(ob[0] * inv, ob[1] * inv), cvtpk(ob[2] * inv, ob[3] * inv)};
    __builtin_amdgcn_raw_buffer_store_b128(pk, rws, WS_PART + (kvh * 32 + j) * 1024 + lane * 16, 0, AUX_SC1);
    const float lam = fmaf(M, SC_LOG2, __builtin_amdgcn_logf(Ls));
    DRAIN();
    if (c16 == 0) __hip_atomic_store((GAS u64*)(wsg + WS_H1F + (kvh * 32 + j) * FS) + h, ((u64)T32 << 32) | f2u(lam), RLX);
  }
  STAMP(4);
  if (LATEW && role >= 2) {
    SB(); RAWBAR(); SB();
    if (w < 4) { u32x4 wst[16]; LOAD_WEIGHTS(wst); STAGE_WEIGHTS(wst); }
    wg_barrier();
  }

  const int rb = lane >> 3, cp = lane & 7;
  if (role == 1) {
    const int h = w >> 1, np = w & 1;
    u32x4 Bf[2][4];
    {
      const int q = (lane >> 2) & 3;
#pragma unroll
      for (int n = 0; n < 2; ++n)
#pragma unroll
        for (int u = 0; u < 4; ++u) {
          const int d0 = h * 128 + 32 * g + 8 * (q >> 1) + 4 * (q & 1) + u, d1 = d0 + 16;
          const int sg = (q >> 1) | ((g & 1) << 1);
          const u32x2 lo = tr_read(L + L_ROLE + (d0 ^ (q & 1)) * 128 + 32 * ((2 * np + n) ^ sg) + (lane & 3) * 8);
          const u32x2 hi = tr_read(L + L_ROLE + (d1 ^ (q & 1)) * 128 + 32 * ((2 * np + n) ^ sg) + (lane & 3) * 8);
          Bf[n][u] = u32x4{lo.x, lo.y, hi.x, hi.y};
        }
    }
    GAS u64* fp = (GAS u64*)(wsg + WS_H1F + (kvh * 32 + 4 * w + ((lane >> 2) & 3)) * FS) + (lane & 3);
    u64 f0; int spins = 0;
    for (;;) {
      f0 = __hip_atomic_load(fp, RLX);
      if (__all((u32)(f0 >> 32) == T32) || ++spins > SPIN_MAX) break;
      MEMFENCE(); __builtin_amdgcn_s_sleep(2);
    }
    MEMFENCE();
    u32x4 pv[4];
#pragma unroll
    for (int i = 0; i < 4; ++i) pv[i] = LDB128(rws, WS_PART + (kvh * 32 + 4 * w + i) * 1024 + lane * 16, 0, AUX_SC1);
    if (lane < 16) *(LAS u32*)(L + L_LAM + ((lane & 3) * 32 + 4 * w + (lane >> 2)) * 4) = (u32)f0;
#pragma unroll
    for (int i = 0; i < 4; ++i) *(LAS u32x4*)(L + L_P + (4 * w + i) * P_STR + lane * 16) = pv[i];
    wg_barrier();
#pragma unroll
    for (int T = 0; T < 2; ++T) {
      const f32x4 lm4 = *(LAS f32x4*)(L + L_LAM + (h * 32 + 16 * T + 4 * g) * 4);
      f32x4 a0 = {0.f, 0.f, 0.f, 0.f}, a1 = {0.f, 0.f, 0.f, 0.f};
#pragma unroll
      for (int u = 0; u < 4; ++u) {
        const u32x4 av = *(LAS u32x4*)(L + L_P + (16 * T + c16) * P_STR + h * 256 + u * 64 + g * 16);
        a0 = mfma32(av, Bf[0][u], a0); a1 = mfma32(av, Bf[1][u], a1);
      }
      const float lm = fmaxf(fmaxf(lm4[0], lm4[1]), fmaxf(lm4[2], lm4[3]));
      float e0 = __builtin_amdgcn_exp2f(lm4[0] - lm), e1 = __builtin_amdgcn_exp2f(lm4[1] - lm), e2 = __builtin_amdgcn_exp2f(lm4[2] - lm), e3 = __builtin_amdgcn_exp2f(lm4[3] - lm);
      const float inv = __builtin_amdgcn_rcpf((e0 + e1) + (e2 + e3));
      e0 *= inv; e1 *= inv; e2 *= inv; e3 *= inv;
      *(LAS float*)(L + L_RED + ((h * 8 + 4 * T + g) * 64 + 32 * np + c16) * 4) = (e0 * a0[0] + e1 * a0[1]) + (e2 * a0[2] + e3 * a0[3]);
      *(LAS float*)(L + L_RED + ((h * 8 + 4 * T + g) * 64 + 32 * np + 16 + c16) * 4) = (e0 * a1[0] + e1 * a1[1]) + (e2 * a1[2] + e3 * a1[3]);
    }
    wg_barrier();
    STAMP(5);
    if (w == 0) {
      f32x4 sa = {0.f, 0.f, 0.f, 0.f}, sb = {0.f, 0.f, 0.f, 0.f};
#pragma unroll
      for (int hh = 0; hh < 4; ++hh) { sa += *(LAS f32x4*)(L + L_RED + ((hh * 8 + rb) * 64 + 8 * cp) * 4); sb += *(LAS f32x4*)(L + L_RED + ((hh * 8 + rb) * 64 + 8 * cp) * 4 + 16); }
      const u32x4 pk = {cvtpk(sa[0], sa[1]), cvtpk(sa[2], sa[3]), cvtpk(sb[0], sb[1]), cvtpk(sb[2], sb[3])};
      __builtin_amdgcn_raw_buffer_store_b128(pk, rws, WS_Y + ((kvh * 8 + rb) * DM + 64 * b + 8 * cp) * 2, 0, AUX_SC1);
      DRAIN();
      if (lane == 0) __hip_atomic_store((GAS u64*)(wsg + WS_H2F + (kvh * 8 + b) * FS), T64, RLX);
    }
    STAMP(6);
  } else if (role == 2) {
    const int mat = w & 1, kq = w >> 1;
    u32x4 Bf[2][4];
    {
      const int q = (lane >> 2) & 3;
#pragma unroll
      for (int n = 0; n < 2; ++n)
#pragma unroll
        for (int u = 0; u < 4; ++u) {
          LAS unsigned char* p = L + L_ROLE + mat * 32768 + (128 * kq + 32 * u + 8 * g + q) * 64 + 32 * (n ^ (g & 1)) + (lane & 3) * 8;
          const u32x2 lo = tr_read(p), hi = tr_read(p + 256);
          Bf[n][u] = u32x4{lo.x, lo.y, hi.x, hi.y};
        }
    }
    const u32x4 xv = LDB128(rx, (w * DM + 8 * lane) * 2, 0, 0);
    if (w == 0) {
      GAS u64* fp = (GAS u64*)(wsg + WS_H2F + lane * FS); int spins = 0;
      for (;;) {
        const u64 f0 = __hip_atomic_load(fp, RLX);
        if (__all(f0 == T64) || ++spins > SPIN_MAX) break;
        MEMFENCE(); __builtin_amdgcn_s_sleep(1);
      }
    }
    wg_barrier();
    STAMP(7);
    u32x4 yv[8];
#pragma unroll
    for (int i = 0; i < 8; ++i) yv[i] = LDB128(rws, WS_Y + ((i * 8 + w) * DM + 8 * lane) * 2, 0, AUX_SC1);
    float v[8];
#pragma unroll
    for (int i = 0; i < 4; ++i) { v[2 * i] = bflo(xv[i]); v[2 * i + 1] = bfhi(xv[i]); }
#pragma unroll
    for (int i = 0; i < 8; ++i) {
#pragma unroll
      for (int e = 0; e < 4; ++e) { v[2 * e] += bflo(yv[i][e]); v[2 * e + 1] += bfhi(yv[i][e]); }
    }
    *(LAS u32x4*)(L + L_A + w * A_STR + lane * 16) = ln_row(v);
    wg_barrier();
    STAMP(8);
    f32x4 a0 = {0.f, 0.f, 0.f, 0.f}, a1 = {0.f, 0.f, 0.f, 0.f};
#pragma unroll
    for (int u = 0; u < 4; ++u) {
      const u32x4 av = *(LAS u32x4*)(L + L_A + (lane & 7) * A_STR + (128 * kq + 32 * u + 8 * g) * 2);
      a0 = mfma32(av, Bf[0][u], a0); a1 = mfma32(av, Bf[1][u], a1);
    }
    if (lane < 32) {
#pragma unroll
      for (int r = 0; r < 4; ++r) {
        *(LAS float*)(L + L_RED + (((kq * 2 + mat) * 8 + 4 * g + r) * 32 + c16) * 4) = a0[r];
        *(LAS float*)(L + L_RED + (((kq * 2 + mat) * 8 + 4 * g + r) * 32 + 16 + c16) * 4) = a1[r];
      }
    }
    wg_barrier();
    if (w == 0) {
      f32x4 ga = {0.f, 0.f, 0.f, 0.f}, up = {0.f, 0.f, 0.f, 0.f};
#pragma unroll
      for (int q = 0; q < 4; ++q) { ga += *(LAS f32x4*)(L + L_RED + (((q * 2 + 0) * 8 + rb) * 32 + 4 * cp) * 4); up += *(LAS f32x4*)(L + L_RED + (((q * 2 + 1) * 8 + rb) * 32 + 4 * cp) * 4); }
      float r[4];
#pragma unroll
      for (int e = 0; e < 4; ++e) {
        const float z = ga[e];
        const float t = z * fmaf(z * z, 0.044715f * 0.7978845608028654f, 0.7978845608028654f);
        const float sg = __builtin_amdgcn_rcpf(1.0f + __builtin_amdgcn_exp2f(t * (-2.0f * 1.4426950408889634f)));
        r[e] = z * sg * up[e];
      }
      __builtin_amdgcn_raw_buffer_store_b64(u32x2{cvtpk(r[0], r[1]), cvtpk(r[2], r[3])}, rws, WS_G + (rb * FF + 32 * cgu + 4 * cp) * 2, 0, AUX_SC1);
      DRAIN();
      if (lane == 0) __hip_atomic_store((GAS u64*)(wsg + WS_H3F + cgu * FS), T64, RLX);
    }
    STAMP(9);
  } else if (role == 3) {
    u32x4 Bf[7];
#pragma unroll
    for (int u = 0; u < 7; ++u) Bf[u] = bfrag(L + L_ROLE + (224 * w + 32 * u) * 32 + trl, 128);
    f32x2 hres = {0.f, 0.f};
    if (w == 0) {
      const u32 xw = *(const GAS u32*)((const GAS unsigned char*)x + (rb * DM + 16 * cdn + 2 * cp) * 2);
      GAS u64* fp = (GAS u64*)(wsg + WS_H2F + ((lane & 7) * 8 + (cdn >> 2)) * FS); int spins = 0;
      for (;;) { const u64 f0 = __hip_atomic_load(fp, RLX); if (__all(f0 == T64) || ++spins > SPIN_MAX) break; MEMFENCE(); __builtin_amdgcn_s_sleep(2); }
      MEMFENCE();
      u32 yy[8];
#pragma unroll
      for (int q = 0; q < 8; ++q) yy[q] = __builtin_amdgcn_raw_buffer_load_b32(rws, WS_Y + ((q * 8 + rb) * DM + 16 * cdn + 2 * cp) * 2, 0, AUX_SC1);
      hres = f32x2{bflo(xw), bfhi(xw)};
#pragma unroll
      for (int q = 0; q < 8; ++q) hres += f32x2{bflo(yy[q]), bfhi(yy[q])};
      GAS u64* gp = (GAS u64*)(wsg + WS_H3F + lane * FS); spins = 0;
      for (;;) {
        const u64 f0 = lane < NGU ? __hip_atomic_load(gp, RLX) : T64;
        if (__all(f0 == T64) || ++spins > SPIN_MAX) break;
        MEMFENCE(); __builtin_amdgcn_s_sleep(1);
      }
      if (cdn == 0 && lane == 0) __hip_atomic_store((GAS u64*)(wsg + WS_E), T64, RLX);
    }
    wg_barrier();
    STAMP(9);
    {
      u32x4 gv[4];
#pragma unroll
      for (int i = 0; i < 4; ++i) gv[i] = LDB128(rws, (i == 3 && w >= 4) ? WS_END : WS_G + ((w + 8 * i) * 64 + lane) * 16, 0, AUX_SC1);
#pragma unroll
      for (int i = 0; i < 4; ++i) { const int ci = (w + 8 * i) * 64 + lane, row = ci / 224; if (i < 3 || w < 4) *(LAS u32x4*)(L + L_P + row * G_STR + (ci - row * 224) * 16) = gv[i]; }
    }
    wg_barrier();
    f32x4 acc = {0.f, 0.f, 0.f, 0.f};
#pragma unroll
    for (int u = 0; u < 7; ++u) acc = mfma32(*(LAS u32x4*)(L + L_P + (lane & 7) * G_STR + ((7 * w + u) * 32 + 8 * g) * 2), Bf[u], acc);
    if (lane < 32) {
#pragma unroll
      for (int r = 0; r < 4; ++r) *(LAS float*)(L + L_RED + ((w * 8 + 4 * g + r) * 16 + c16) * 4) = acc[r];
    }
    wg_barrier();
    if (w == 0) {
      f32x2 s = hres;
#pragma unroll
      for (int ww = 0; ww < 8; ++ww) s += *(LAS f32x2*)(L + L_RED + ((ww * 8 + rb) * 16 + 2 * cp) * 4);
      *(GAS u32*)((GAS unsigned char*)out + (rb * DM + 16 * cdn + 2 * cp) * 2) = cvtpk(s[0], s[1]);
    }
    STAMP(8);
  }
  STAMP_FLUSH();
}

extern "C" void kernel_launch(void* const* d_in, const int* in_sizes, int n_in, void* d_out, int out_size, void* d_ws, size_t ws_size, hipStream_t stream) {
  fused_decode_block<<<NWG, NTHR, 0, stream>>>((const u16*)d_in[0], (const u16*)d_in[1], (const u16*)d_in[2], (const u16*)d_in[3], (const u16*)d_in[4],
                                              (const u16*)d_in[6], (const u16*)d_in[7], (const u16*)d_in[8], (u16*)d_out, (unsigned char*)d_ws);
}
```

```cpp
#include <hip/hip_runtime.h>
#include <hip/hip_bf16.h>
#include <stdint.h>

typedef unsigned short u16;
typedef unsigned u32;
typedef unsigned long long u64;
typedef u32 u32x4 __attribute__((ext_vector_type(4)));
typedef u32 u32x2 __attribute__((ext_vector_type(2)));
typedef float f32x4 __attribute__((ext_vector_type(4)));
typedef float f32x2 __attribute__((ext_vector_type(2)));
typedef short s16x8 __attribute__((ext_vector_type(8)));
typedef short s16x4 __attribute__((ext_vector_type(4)));
typedef __bf16 bf16x8_t __attribute__((ext_vector_type(8)));
#define GAS __attribute__((address_space(1)))
#define LAS __attribute__((address_space(3)))
#define RLX __ATOMIC_RELAXED, __HIP_MEMORY_SCOPE_AGENT

#define QPOS 0
#define HOLD 1
#define PASSES 1
#define SLEEP0 8
#define QSLEEP 4
#define LATEW 1
#define VB 8
#define KB 8
#define WAUX 2
#define DIAG 0
#define STAMPS 0

constexpr int NB = 8, DM = 512, HQ = 32, HKV = 8, SK = 8192, DH = 128, FF = 1792;
constexpr int NWG = 256, NTHR = 512;
constexpr int NGU = FF / 32;
constexpr int TOK_WG = SK / 4;
constexpr int NIT = TOK_WG / 16 / 8;
constexpr float SC_LOG2 = 0.08838834764831845f * 1.4426950408889634f;
constexpr float LN_EPS = 1e-5f;
constexpr int AUX_NT = 2, AUX_SC1 = 16;
constexpr int SPIN_MAX = 1 << 16;

constexpr int FS = 256;
constexpr int WS_E    = 0;
constexpr int WS_H1F  = 4096;
constexpr int WS_H2F  = WS_H1F + 256 * FS;
constexpr int WS_H3F  = WS_H2F + 64 * FS;
constexpr int WS_QG   = WS_H3F + 64 * FS;
constexpr int WS_PART = WS_QG + HKV * NB * 4 * 512 * 8;
constexpr int WS_Y    = WS_PART + HKV * 32 * 4 * 128 * 2;
constexpr int WS_G    = WS_Y + HKV * NB * DM * 2;
constexpr int WS_END  = WS_G + NB * FF * 2;
constexpr int WS_STAMP = 2 << 20;

constexpr int L_ROLE = 0;
constexpr int L_WQ = 0;
constexpr int L_MRG = 65536;
constexpr int A_STR = 1040;
constexpr int L_A = L_MRG + 16384;
constexpr int L_RED = L_A + 8 * A_STR;
constexpr int L_Q = L_RED + 8192;
constexpr int L_ST = L_Q + 1024;
constexpr int L_LAM = L_ST + 256;
constexpr int P_STR = 1040;
constexpr int L_P = L_LAM + 512;
constexpr int G_STR = 3600;
constexpr int LDS_BYTES = L_P + 32 * P_STR;

typedef __bf16 bf16x2_t __attribute__((ext_vector_type(2)));
__device__ __forceinline__ u32 cvtpk(float lo, float hi) { const bf16x2_t r = {(__bf16)lo, (__bf16)hi}; return __builtin_bit_cast(u32, r); }
__device__ __forceinline__ float u2f(u32 v) { return __builtin_bit_cast(float, v); }
__device__ __forceinline__ u32 f2u(float v) { return __builtin_bit_cast(u32, v); }
__device__ __forceinline__ float bflo(u32 v) { return __builtin_bit_cast(float, v << 16); }
__device__ __forceinline__ float bfhi(u32 v) { return __builtin_bit_cast(float, v & 0xffff0000u); }
template <int CTRL> __device__ __forceinline__ float dpp(float x) { return __builtin_bit_cast(float, __builtin_amdgcn_mov_dpp(__builtin_bit_cast(int, x), CTRL, 0xf, 0xf, true)); }
__device__ __forceinline__ float xrow_max(float x) {
  auto s = __builtin_amdgcn_permlane16_swap(f2u(x), f2u(x), false, false);
  x = fmaxf(u2f(s[0]), u2f(s[1]));
  auto t = __builtin_amdgcn_permlane32_swap(f2u(x), f2u(x), false, false);
  return fmaxf(u2f(t[0]), u2f(t[1]));
}
__device__ __forceinline__ float xrow_sum(float x) {
  auto s = __builtin_amdgcn_permlane16_swap(f2u(x), f2u(x), false, false);
  x = u2f(s[0]) + u2f(s[1]);
  auto t = __builtin_amdgcn_permlane32_swap(f2u(x), f2u(x), false, false);
  return u2f(t[0]) + u2f(t[1]);
}
__device__ __forceinline__ float wave_sum(float x) {
  x += dpp<0xB1>(x); x += dpp<0x4E>(x); x += dpp<0x141>(x); x += dpp<0x140>(x);
  return xrow_sum(x);
}
__device__ __forceinline__ f32x4 mfma32(u32x4 a, u32x4 b, f32x4 c) {
  return __builtin_amdgcn_mfma_f32_16x16x32_bf16(__builtin_bit_cast(bf16x8_t, a), __builtin_bit_cast(bf16x8_t, b), c, 0, 0, 0);
}
__device__ __forceinline__ f32x4 mfma16(u32x2 a, u32x2 b, f32x4 c) {
  return __builtin_amdgcn_mfma_f32_16x16x16bf16_1k(__builtin_bit_cast(s16x4, a), __builtin_bit_cast(s16x4, b), c, 0, 0, 0);
}
typedef short v4i16_t __attribute__((ext_vector_type(4)));
__device__ __forceinline__ u32x2 tr_read(LAS unsigned char* p) {
  return __builtin_bit_cast(u32x2, __builtin_amdgcn_ds_read_tr16_b64_v4i16((LAS v4i16_t*)p));
}
__device__ __forceinline__ u32x4 bfrag(LAS unsigned char* p, int second_off) {
  const u32x2 a = tr_read(p), b = tr_read(p + second_off);
  return u32x4{a.x, a.y, b.x, b.y};
}
__device__ __forceinline__ __amdgpu_buffer_rsrc_t mkrs(const void* p, u32 bytes) { return __builtin_amdgcn_make_buffer_rsrc((void*)p, 0, bytes, 0x00020000); }
template <int V> struct ic { static constexpr int value = V; };
#define LDB128(rs, vo, so, aux) __builtin_amdgcn_raw_buffer_load_b128(rs, vo, so, aux)
#define SB() __builtin_amdgcn_sched_barrier(0)
#define DRAIN() asm volatile("s_waitcnt vmcnt(0)" ::: "memory")
#define MEMFENCE() asm volatile("" ::: "memory")
#define RAWBAR() asm volatile("s_barrier" ::: "memory")
__device__ __forceinline__ void wg_barrier() { asm volatile("s_waitcnt lgkmcnt(0)\n\ts_barrier" ::: "memory"); }

__device__ __forceinline__ u32x4 ln_row(const float (&v)[8]) {
  float s = ((v[0] + v[1]) + (v[2] + v[3])) + ((v[4] + v[5]) + (v[6] + v[7]));
  const float mean = wave_sum(s) * (1.0f / DM);
  float d[8], q = 0.f;
#pragma unroll
  for (int i = 0; i < 8; ++i) { d[i] = v[i] - mean; q = fmaf(d[i], d[i], q); }
  const float var = wave_sum(q) * (1.0f / DM);
  const float rs = __builtin_amdgcn_rsqf(var + LN_EPS);
  return u32x4{cvtpk(d[0] * rs, d[1] * rs), cvtpk(d[2] * rs, d[3] * rs), cvtpk(d[4] * rs, d[5] * rs), cvtpk(d[6] * rs, d[7] * rs)};
}

#define STAMP(i) do {} while (0)
#define STAMP_FLUSH() do {} while (0)

__global__ void __launch_bounds__(NTHR, 2)
fused_decode_block(const u16* x_, const u16* Kc_, const u16* Vc_, const u16* wq_, const u16* wo_,
                   const u16* wg_, const u16* wu_, const u16* wd_, u16* out_, unsigned char* ws_) {
  const u16 *x = x_, *Kc = Kc_, *Vc = Vc_, *wq = wq_, *wo = wo_, *wg = wg_, *wu = wu_, *wd = wd_; u16* out = out_; unsigned char* ws = ws_;
  asm volatile("" : "+s"(x), "+s"(Kc), "+s"(Vc), "+s"(wq), "+s"(wo), "+s"(wg), "+s"(wu), "+s"(wd), "+s"(out), "+s"(ws));
  __shared__ __attribute__((aligned(16))) unsigned char lds_raw[LDS_BYTES];
  LAS unsigned char* L = (LAS unsigned char*)lds_raw;
  const int tid = threadIdx.x, lane = tid & 63, w = __builtin_amdgcn_readfirstlane(tid >> 6);
  const int blk = blockIdx.x, kvh = blk & 7, j = blk >> 3, b = j >> 2, sp = j & 3;
  const int g = lane >> 4, c16 = lane & 15;
  GAS unsigned char* wsg = (GAS unsigned char*)ws;
  STAMP(0);

  u64 E;
  asm volatile("s_load_dwordx2 %0, %1, 0x0" : "=s"(E) : "s"(ws));
  const __amdgpu_buffer_rsrc_t rx = mkrs(x, NB * DM * 2);
  const __amdgpu_buffer_rsrc_t rwq = mkrs(wq, DM * HQ * DH * 2);
  const size_t kvbase = ((size_t)(b * HKV + kvh) * SK + (size_t)sp * TOK_WG) * DH;
  const __amdgpu_buffer_rsrc_t rk = mkrs(Kc + kvbase, TOK_WG * DH * 2);
  const __amdgpu_buffer_rsrc_t rv = mkrs(Vc + kvbase, TOK_WG * DH * 2);
  const __amdgpu_buffer_rsrc_t rws = mkrs(ws, WS_END);

  const int trl = (8 * g + ((lane >> 2) & 3)) * 32 + (lane & 3) * 8;
  const int jj = 3 * b + sp - 1;
  const int role = sp == 0 ? 1 : (jj < 7 ? 2 : (jj < 11 ? 3 : 0));
  const int cgu = kvh * 7 + jj, cdn = kvh * 4 + (jj - 7);
#define LOAD_WEIGHTS(wst) do { \
      const u16* p0 = role == 1 ? wo : (role == 2 ? wg : wd); \
      const u16* p1 = role == 2 ? wu : p0; \
      const u32 n0b = role == 0 ? 0u : (role == 1 ? (u32)(HQ * DH * DM * 2) : (u32)(DM * FF * 2)); \
      const u32 n1b = role == 3 ? 0u : n0b; \
      const __amdgpu_buffer_rsrc_t r0 = mkrs(p0, n0b), r1 = mkrs(p1, n0b), r2 = mkrs(p1, n1b); \
      const int v0o = role == 1 ? ((kvh * 512 + 128 * w + (lane >> 3)) * DM + 64 * b + 8 * (lane & 7)) * 2 \
                    : role == 2 ? ((128 * w + (lane >> 2)) * FF + 32 * cgu + 8 * (lane & 3)) * 2 \
                                : ((448 * w + (lane >> 1)) * DM + 16 * cdn + 8 * (lane & 1)) * 2; \
      const int st = role == 1 ? 8 * DM * 2 : role == 2 ? 16 * FF * 2 : 32 * DM * 2; \
      const int v8o = role == 2 ? v0o : v0o + 8 * st; \
      _Pragma("unroll") for (int i = 0; i < 8; ++i) wst[i] = LDB128(r0, v0o + i * st, 0, WAUX); \
      _Pragma("unroll") for (int i = 0; i < 6; ++i) wst[8 + i] = LDB128(r1, v8o + i * st, 0, WAUX); \
      _Pragma("unroll") for (int i = 6; i < 8; ++i) wst[8 + i] = LDB128(r2, v8o + i * st, 0, WAUX); \
      SB(); } while (0)
#define STAGE_WEIGHTS(wst) do { \
    if (role == 1) { \
      _Pragma("unroll") for (int i = 0; i < 16; ++i) { const int r = 128 * w + 8 * i + (lane >> 3), ch = lane & 7; \
        *(LAS u32x4*)(L + L_ROLE + (r ^ ((r >> 2) & 1)) * 128 + 32 * ((ch >> 1) ^ (((r >> 3) & 1) | (((r >> 5) & 1) << 1))) + 16 * (ch & 1)) = wst[i]; } \
    } else if (role == 2) { \
      _Pragma("unroll") for (int i = 0; i < 16; ++i) { const int r = 128 * w + 16 * (i & 7) + (lane >> 2), ch = lane & 3; \
        *(LAS u32x4*)(L + L_ROLE + (i >> 3) * 32768 + r * 64 + 32 * ((ch >> 1) ^ ((r >> 3) & 1)) + 16 * (ch & 1)) = wst[i]; } \
    } else if (role == 3) { \
      _Pragma("unroll") for (int i = 0; i < 14; ++i) *(LAS u32x4*)(L + L_ROLE + (448 * w + 32 * i) * 32 + lane * 16) = wst[i]; \
    } } while (0)
  u64 T64;
  if (w < 4) {
    const u32x4 xr0 = LDB128(rx, (2 * w * DM + 8 * lane) * 2, 0, 0), xr1 = LDB128(rx, ((2 * w + 1) * DM + 8 * lane) * 2, 0, 0);
    u32x4 wqr[4];
#pragma unroll
    for (int i = 0; i < 4; ++i) wqr[i] = LDB128(rwq, ((128 * sp + 32 * w + 8 * i + (lane >> 3)) * (HQ * DH) + kvh * 512 + 64 * b + 8 * (lane & 7)) * 2, 0, WAUX);
    SB(); RAWBAR(); SB();
    float v0[8], v1[8];
#pragma unroll
    for (int i = 0; i < 4; ++i) { v0[2 * i] = bflo(xr0[i]); v0[2 * i + 1] = bfhi(xr0[i]); v1[2 * i] = bflo(xr1[i]); v1[2 * i + 1] = bfhi(xr1[i]); }
    const u32x4 n0 = ln_row(v0), n1 = ln_row(v1);
    *(LAS u32x4*)(L + L_A + 2 * w * A_STR + lane * 16) = n0;
    *(LAS u32x4*)(L + L_A + (2 * w + 1) * A_STR + lane * 16) = n1;
#pragma unroll
    for (int i = 0; i < 4; ++i) *(LAS u32x4*)(L + L_WQ + (32 * w + 8 * i + (lane >> 3)) * 144 + (lane & 7) * 16) = wqr[i];
    STAMP(12);
    wg_barrier();
    STAMP(13);
    const u32x4 a = *(LAS u32x4*)(L + L_A + (lane & 7) * A_STR + (128 * sp + 32 * w + 8 * g) * 2);
#pragma unroll
    for (int nt = 0; nt < 4; ++nt) {
      LAS unsigned char* p = L + L_WQ + (32 * w + 8 * g + ((lane >> 2) & 3)) * 144 + nt * 32 + (lane & 3) * 8;
      const u32x2 lo = tr_read(p), hi = tr_read(p + 4 * 144);
      const f32x4 acc = mfma32(a, u32x4{lo.x, lo.y, hi.x, hi.y}, f32x4{0.f, 0.f, 0.f, 0.f});
      if (lane < 32) {
#pragma unroll
        for (int r = 0; r < 4; ++r) *(LAS float*)(L + L_P + ((w * 8 + 4 * g + r) * 64 + 16 * nt + c16) * 4) = acc[r];
      }
    }
    wg_barrier();
    STAMP(14);
    asm volatile("s_waitcnt lgkmcnt(0)" : "+s"(E));
    T64 = E + 1;
    const u64 T40 = T64 & 0xFFFFFFFFFFull;
#pragma unroll
    for (int i = 0; i < 2; ++i) {
      const int o = tid + 256 * i;
      float sum = 0.f;
#pragma unroll
      for (int ww = 0; ww < 4; ++ww) sum += *(LAS float*)(L + L_P + (ww * 512 + o) * 4);
      const u32 sv = f2u(sum) + 0x80u;
      __hip_atomic_store((GAS u64*)(wsg + WS_QG) + ((kvh * 8 + (o >> 6)) * 4 + sp) * 512 + 64 * b + (o & 63), (T40 << 24) | (u64)(sv >> 8), RLX);
    }
    STAMP(1);
    DRAIN();
    if (QSLEEP) __builtin_amdgcn_s_sleep(QSLEEP);
    STAMP(10);
    {
      GAS u64* qgp = (GAS u64*)(wsg + WS_QG) + (kvh * 8 + b) * 4 * 512 + tid;
      u64 qv[8]; int spins = 0;
      for (;;) {
        bool ok = true;
#pragma unroll
        for (int i = 0; i < 8; ++i) qv[i] = __hip_atomic_load(qgp + (i & 3) * 512 + (i >> 2) * 256, RLX);
#pragma unroll
        for (int i = 0; i < 8; ++i) ok &= (qv[i] >> 24) == T40;
        if (__all(ok) || ++spins > SPIN_MAX) break;
        MEMFENCE(); __builtin_amdgcn_s_sleep(2);
      }
      const float qa = (u2f((u32)qv[0] << 8) + u2f((u32)qv[1] << 8)) + (u2f((u32)qv[2] << 8) + u2f((u32)qv[3] << 8));
      const float qb = (u2f((u32)qv[4] << 8) + u2f((u32)qv[5] << 8)) + (u2f((u32)qv[6] << 8) + u2f((u32)qv[7] << 8));
      *(LAS u16*)(L + L_Q + tid * 2) = (u16)cvtpk(qa, qa);
      *(LAS u16*)(L + L_Q + 512 + tid * 2) = (u16)cvtpk(qb, qb);
    }
    STAMP(15);
    wg_barrier();
    SB();
    if (role == 1 || !LATEW) { u32x4 wst[16]; LOAD_WEIGHTS(wst); STAGE_WEIGHTS(wst); }
    wg_barrier();
  } else {
    const int sw = w - 4;
    const int koff = (4 * (c16 & 3) + (c16 >> 2)) * 256 + g * 16, voff = lane * 16;
    const bool vkeep = b >= VB;
#define ISSUE_K(slot, it) do { const int so_ = ((it) * 4 + sw) * 4096; \
    _Pragma("unroll") for (int u = 0; u < 4; ++u) Kb[slot][u] = LDB128(rk, koff + u * 64, so_, KAUX); SB(); } while (0)
#define ISSUE_V(slot, it) do { const int so_ = ((it) * 4 + sw) * 4096; \
    _Pragma("unroll") for (int u = 0; u < 4; ++u) Vb[slot][u] = LDB128(rv, voff + u * 1024, so_, VAUX); SB(); } while (0)
#define ISSUE(slot, it) do { ISSUE_K(slot, it); ISSUE_V(slot, it); } while (0)
    float mrun = -INFINITY, lrun = 0.f;
    f32x4 O1 = {0.f, 0.f, 0.f, 0.f}, O2 = {0.f, 0.f, 0.f, 0.f};
    auto stream = [&](auto kaux, auto vaux) __attribute__((always_inline)) {
    constexpr int KAUX = decltype(kaux)::value, VAUX = decltype(vaux)::value;
    u32x4 Kb[3][4], Vb[3][4];
    RAWBAR();
    if (SLEEP0) __builtin_amdgcn_s_sleep(SLEEP0);
    SB();
    ISSUE(0, 0);
    RAWBAR(); SB();
    ISSUE_V(1, 1);
    RAWBAR(); SB();
    ISSUE_K(1, 1);
    asm volatile("s_waitcnt vmcnt(4)" ::: "memory"); SB();
    ISSUE_K(2, 2);
    asm volatile("s_waitcnt vmcnt(4)" ::: "memory"); SB();
    ISSUE_V(2, 2);
    wg_barrier();
    STAMP(2);
    u32x4 Qf[4];
#pragma unroll
    for (int u = 0; u < 4; ++u) Qf[u] = *(LAS u32x4*)(L + L_Q + ((lane & 3) * 128 + 32 * u + 8 * g) * 2);
    const int jx = c16 >> 2;
    const u32 m0k = jx == 0 ? 0x0000FFFFu : jx == 1 ? 0xFFFF0000u : 0u;
    const u32 m1k = jx == 2 ? 0x0000FFFFu : jx == 3 ? 0xFFFF0000u : 0u;
#define PROCESS(slot) do { \
    f32x4 s_ = {0.f, 0.f, 0.f, 0.f}; \
    _Pragma("unroll") for (int u = 0; u < 4; ++u) s_ = mfma32(Kb[slot][u], Qf[u], s_); \
    const float tm_ = xrow_max(fmaxf(fmaxf(s_[0], s_[1]), fmaxf(s_[2], s_[3]))); \
    if (__any(tm_ > mrun)) { \
      const float mn_ = fmaxf(mrun, tm_), al_ = __builtin_amdgcn_exp2f((mrun - mn_) * SC_LOG2); \
      lrun *= al_; mrun = mn_; \
      const float a0_ = dpp<0x00>(al_), a1_ = dpp<0x55>(al_), a2_ = dpp<0xAA>(al_), a3_ = dpp<0xFF>(al_); \
      O1[0] *= a0_; O1[1] *= a1_; O1[2] *= a2_; O1[3] *= a3_; O2[0] *= a0_; O2[1] *= a1_; O2[2] *= a2_; O2[3] *= a3_; \
    } \
    const float mc_ = mrun * SC_LOG2; \
    _Pragma("unroll") for (int i = 0; i < 4; ++i) { \
      const float p_ = __builtin_amdgcn_exp2f(fmaf(s_[i], SC_LOG2, -mc_)); lrun += p_; \
      const u32 pv_ = cvtpk(p_, p_); const u32x2 a_ = {pv_ & m0k, pv_ & m1k}; \
      O1 = mfma16(a_, u32x2{Vb[slot][i].x, Vb[slot][i].y}, O1); O2 = mfma16(a_, u32x2{Vb[slot][i].z, Vb[slot][i].w}, O2); \
    } SB(); } while (0)
#pragma unroll 1
    for (int it = 0; it < 27; it += 3) { PROCESS(0); ISSUE(0, it + 3); PROCESS(1); ISSUE(1, it + 4); PROCESS(2); ISSUE(2, it + 5); }
    PROCESS(0); ISSUE(0, 30); PROCESS(1); ISSUE(1, 31); PROCESS(2); PROCESS(0); PROCESS(1);
    };
    if (vkeep) stream(ic<0>{}, ic<0>{}); else stream(ic<AUX_NT>{}, ic<AUX_NT>{});
    asm volatile("s_waitcnt lgkmcnt(0)" : "+s"(E));
    T64 = E + 1;
    STAMP(3);
    lrun = xrow_sum(lrun);
#pragma unroll
    for (int h = 0; h < 4; ++h) *(LAS f32x2*)(L + L_MRG + ((sw * 4 + h) * 64 + lane) * 8) = f32x2{O1[h], O2[h]};
    if (lane < 4) { *(LAS float*)(L + L_ST + (lane * 4 + sw) * 4) = mrun; *(LAS float*)(L + L_ST + 64 + (lane * 4 + sw) * 4) = lrun; }
    wg_barrier();
  }
  const u32 T32 = (u32)T64;

  if (w == 0) {
    const int h = g;
    const f32x4 ma = *(LAS f32x4*)(L + L_ST + h * 16), la = *(LAS f32x4*)(L + L_ST + 64 + h * 16);
    const float M = fmaxf(fmaxf(ma[0], ma[1]), fmaxf(ma[2], ma[3]));
    float Ls = 0.f;
    f32x4 oa = {0.f, 0.f, 0.f, 0.f}, ob = {0.f, 0.f, 0.f, 0.f};
#pragma unroll
    for (int ww = 0; ww < 4; ++ww) {
      const float f = __builtin_amdgcn_exp2f((ma[ww] - M) * SC_LOG2);
      Ls = fmaf(f, la[ww], Ls);
      oa += f * *(LAS f32x4*)(L + L_MRG + ((ww * 4 + h) * 64 + 4 * c16) * 8);
      ob += f * *(LAS f32x4*)(L + L_MRG + ((ww * 4 + h) * 64 + 4 * c16) * 8 + 16);
    }
    const float inv = __builtin_amdgcn_rcpf(Ls);
    const u32x4 pk = {cvtpk(oa[0] * inv, oa[1] * inv), cvtpk(oa[2] * inv, oa[3] * inv), cvtpk(ob[0] * inv, ob[1] * inv), cvtpk(ob[2] * inv, ob[3] * inv)};
    __builtin_amdgcn_raw_buffer_store_b128(pk, rws, WS_PART + (kvh * 32 + j) * 1024 + lane * 16, 0, AUX_SC1);
    const float lam = fmaf(M, SC_LOG2, __builtin_amdgcn_logf(Ls));
    DRAIN();
    if (c16 == 0) __hip_atomic_store((GAS u64*)(wsg + WS_H1F + (kvh * 32 + j) * FS) + h, ((u64)T32 << 32) | f2u(lam), RLX);
  }
  STAMP(4);
  if (LATEW && role >= 2) {
    SB(); RAWBAR(); SB();
    if (w < 4) { u32x4 wst[16]; LOAD_WEIGHTS(wst); STAGE_WEIGHTS(wst); }
    wg_barrier();
  }

  const int rb = lane >> 3, cp = lane & 7;
  if (role == 1) {
    const int h = w >> 1, np = w & 1;
    u32x4 Bf[2][4];
    {
      const int q = (lane >> 2) & 3;
#pragma unroll
      for (int n = 0; n < 2; ++n)
#pragma unroll
        for (int u = 0; u < 4; ++u) {
          const int d0 = h * 128 + 32 * g + 8 * (q >> 1) + 4 * (q & 1) + u, d1 = d0 + 16;
          const int sg = (q >> 1) | ((g & 1) << 1);
          const u32x2 lo = tr_read(L + L_ROLE + (d0 ^ (q & 1)) * 128 + 32 * ((2 * np + n) ^ sg) + (lane & 3) * 8);
          const u32x2 hi = tr_read(L + L_ROLE + (d1 ^ (q & 1)) * 128 + 32 * ((2 * np + n) ^ sg) + (lane & 3) * 8);
          Bf[n][u] = u32x4{lo.x, lo.y, hi.x, hi.y};
        }
    }
    GAS u64* fp = (GAS u64*)(wsg + WS_H1F + (kvh * 32 + 4 * w + ((lane >> 2) & 3)) * FS) + (lane & 3);
    u64 f0; int spins = 0;
    for (;;) {
      f0 = __hip_atomic_load(fp, RLX);
      if (__all((u32)(f0 >> 32) == T32) || ++spins > SPIN_MAX) break;
      MEMFENCE(); __builtin_amdgcn_s_sleep(2);
    }
    MEMFENCE();
    u32x4 pv[4];
#pragma unroll
    for (int i = 0; i < 4; ++i) pv[i] = LDB128(rws, WS_PART + (kvh * 32 + 4 * w + i) * 1024 + lane * 16, 0, AUX_SC1);
    if (lane < 16) *(LAS u32*)(L + L_LAM + ((lane & 3) * 32 + 4 * w + (lane >> 2)) * 4) = (u32)f0;
#pragma unroll
    for (int i = 0; i < 4; ++i) *(LAS u32x4*)(L + L_P + (4 * w + i) * P_STR + lane * 16) = pv[i];
    wg_barrier();
#pragma unroll
    for (int T = 0; T < 2; ++T) {
      const f32x4 lm4 = *(LAS f32x4*)(L + L_LAM + (h * 32 + 16 * T + 4 * g) * 4);
      f32x4 a0 = {0.f, 0.f, 0.f, 0.f}, a1 = {0.f, 0.f, 0.f, 0.f};
#pragma unroll
      for (int u = 0; u < 4; ++u) {
        const u32x4 av = *(LAS u32x4*)(L + L_P + (16 * T + c16) * P_STR + h * 256 + u * 64 + g * 16);
        a0 = mfma32(av, Bf[0][u], a0); a1 = mfma32(av, Bf[1][u], a1);
      }
      const float lm = fmaxf(fmaxf(lm4[0], lm4[1]), fmaxf(lm4[2], lm4[3]));
      float e0 = __builtin_amdgcn_exp2f(lm4[0] - lm), e1 = __builtin_amdgcn_exp2f(lm4[1] - lm), e2 = __builtin_amdgcn_exp2f(lm4[2] - lm), e3 = __builtin_amdgcn_exp2f(lm4[3] - lm);
      const float inv = __builtin_amdgcn_rcpf((e0 + e1) + (e2 + e3));
      e0 *= inv; e1 *= inv; e2 *= inv; e3 *= inv;
      *(LAS float*)(L + L_RED + ((h * 8 + 4 * T + g) * 64 + 32 * np + c16) * 4) = (e0 * a0[0] + e1 * a0[1]) + (e2 * a0[2] + e3 * a0[3]);
      *(LAS float*)(L + L_RED + ((h * 8 + 4 * T + g) * 64 + 32 * np + 16 + c16) * 4) = (e0 * a1[0] + e1 * a1[1]) + (e2 * a1[2] + e3 * a1[3]);
    }
    wg_barrier();
    STAMP(5);
    if (w == 0) {
      f32x4 sa = {0.f, 0.f, 0.f, 0.f}, sb = {0.f, 0.f, 0.f, 0.f};
#pragma unroll
      for (int hh = 0; hh < 4; ++hh) { sa += *(LAS f32x4*)(L + L_RED + ((hh * 8 + rb) * 64 + 8 * cp) * 4); sb += *(LAS f32x4*)(L + L_RED + ((hh * 8 + rb) * 64 + 8 * cp) * 4 + 16); }
      const u32x4 pk = {cvtpk(sa[0], sa[1]), cvtpk(sa[2], sa[3]), cvtpk(sb[0], sb[1]), cvtpk(sb[2], sb[3])};
      __builtin_amdgcn_raw_buffer_store_b128(pk, rws, WS_Y + ((kvh * 8 + rb) * DM + 64 * b + 8 * cp) * 2, 0, AUX_SC1);
      DRAIN();
      if (lane == 0) __hip_atomic_store((GAS u64*)(wsg + WS_H2F + (kvh * 8 + b) * FS), T64, RLX);
    }
    STAMP(6);
  } else if (role == 2) {
    const int mat = w & 1, kq = w >> 1;
    u32x4 Bf[2][4];
    {
      const int q = (lane >> 2) & 3;
#pragma unroll
      for (int n = 0; n < 2; ++n)
#pragma unroll
        for (int u = 0; u < 4; ++u) {
          LAS unsigned char* p = L + L_ROLE + mat * 32768 + (128 * kq + 32 * u + 8 * g + q) * 64 + 32 * (n ^ (g & 1)) + (lane & 3) * 8;
          const u32x2 lo = tr_read(p), hi = tr_read(p + 256);
          Bf[n][u] = u32x4{lo.x, lo.y, hi.x, hi.y};
        }
    }
    const u32x4 xv = LDB128(rx, (w * DM + 8 * lane) * 2, 0, 0);
    if (w == 0) {
      GAS u64* fp = (GAS u64*)(wsg + WS_H2F + lane * FS); int spins = 0;
      for (;;) {
        const u64 f0 = __hip_atomic_load(fp, RLX);
        if (__all(f0 == T64) || ++spins > SPIN_MAX) break;
        MEMFENCE(); __builtin_amdgcn_s_sleep(1);
      }
    }
    wg_barrier();
    STAMP(7);
    u32x4 yv[8];
#pragma unroll
    for (int i = 0; i < 8; ++i) yv[i] = LDB128(rws, WS_Y + ((i * 8 + w) * DM + 8 * lane) * 2, 0, AUX_SC1);
    float v[8];
#pragma unroll
    for (int i = 0; i < 4; ++i) { v[2 * i] = bflo(xv[i]); v[2 * i + 1] = bfhi(xv[i]); }
#pragma unroll
    for (int i = 0; i < 8; ++i) {
#pragma unroll
      for (int e = 0; e < 4; ++e) { v[2 * e] += bflo(yv[i][e]); v[2 * e + 1] += bfhi(yv[i][e]); }
    }
    *(LAS u32x4*)(L + L_A + w * A_STR + lane * 16) = ln_row(v);
    wg_barrier();
    STAMP(8);
    f32x4 a0 = {0.f, 0.f, 0.f, 0.f}, a1 = {0.f, 0.f, 0.f, 0.f};
#pragma unroll
    for (int u = 0; u < 4; ++u) {
      const u32x4 av = *(LAS u32x4*)(L + L_A + (lane & 7) * A_STR + (128 * kq + 32 * u + 8 * g) * 2);
      a0 = mfma32(av, Bf[0][u], a0); a1 = mfma32(av, Bf[1][u], a1);
    }
    if (lane < 32) {
#pragma unroll
      for (int r = 0; r < 4; ++r) {
        *(LAS float*)(L + L_RED + (((kq * 2 + mat) * 8 + 4 * g + r) * 32 + c16) * 4) = a0[r];
        *(LAS float*)(L + L_RED + (((kq * 2 + mat) * 8 + 4 * g + r) * 32 + 16 + c16) * 4) = a1[r];
      }
    }
    wg_barrier();
    if (w == 0) {
      f32x4 ga = {0.f, 0.f, 0.f, 0.f}, up = {0.f, 0.f, 0.f, 0.f};
#pragma unroll
      for (int q = 0; q < 4; ++q) { ga += *(LAS f32x4*)(L + L_RED + (((q * 2 + 0) * 8 + rb) * 32 + 4 * cp) * 4); up += *(LAS f32x4*)(L + L_RED + (((q * 2 + 1) * 8 + rb) * 32 + 4 * cp) * 4); }
      float r[4];
#pragma unroll
      for (int e = 0; e < 4; ++e) {
        const float z = ga[e];
        const float t = z * fmaf(z * z, 0.044715f * 0.7978845608028654f, 0.7978845608028654f);
        const float sg = __builtin_amdgcn_rcpf(1.0f + __builtin_amdgcn_exp2f(t * (-2.0f * 1.4426950408889634f)));
        r[e] = z * sg * up[e];
      }
      __builtin_amdgcn_raw_buffer_store_b64(u32x2{cvtpk(r[0], r[1]), cvtpk(r[2], r[3])}, rws, WS_G + (rb * FF + 32 * cgu + 4 * cp) * 2, 0, AUX_SC1);
      DRAIN();
      if (lane == 0) __hip_atomic_store((GAS u64*)(wsg + WS_H3F + cgu * FS), T64, RLX);
    }
    STAMP(9);
  } else if (role == 3) {
    u32x4 Bf[7];
#pragma unroll
    for (int u = 0; u < 7; ++u) Bf[u] = bfrag(L + L_ROLE + (224 * w + 32 * u) * 32 + trl, 128);
    f32x2 hres = {0.f, 0.f};
    if (w == 0) {
      const u32 xw = *(const GAS u32*)((const GAS unsigned char*)x + (rb * DM + 16 * cdn + 2 * cp) * 2);
      GAS u64* fp = (GAS u64*)(wsg + WS_H2F + ((lane & 7) * 8 + (cdn >> 2)) * FS); int spins = 0;
      for (;;) { const u64 f0 = __hip_atomic_load(fp, RLX); if (__all(f0 == T64) || ++spins > SPIN_MAX) break; MEMFENCE(); __builtin_amdgcn_s_sleep(2); }
      MEMFENCE();
      u32 yy[8];
#pragma unroll
      for (int q = 0; q < 8; ++q) yy[q] = __builtin_amdgcn_raw_buffer_load_b32(rws, WS_Y + ((q * 8 + rb) * DM + 16 * cdn + 2 * cp) * 2, 0, AUX_SC1);
      hres = f32x2{bflo(xw), bfhi(xw)};
#pragma unroll
      for (int q = 0; q < 8; ++q) hres += f32x2{bflo(yy[q]), bfhi(yy[q])};
      GAS u64* gp = (GAS u64*)(wsg + WS_H3F + lane * FS); spins = 0;
      for (;;) {
        const u64 f0 = lane < NGU ? __hip_atomic_load(gp, RLX) : T64;
        if (__all(f0 == T64) || ++spins > SPIN_MAX) break;
        MEMFENCE(); __builtin_amdgcn_s_sleep(1);
      }
      if (cdn == 0 && lane == 0) __hip_atomic_store((GAS u64*)(wsg + WS_E), T64, RLX);
    }
    wg_barrier();
    STAMP(9);
    {
      u32x4 gv[4];
#pragma unroll
      for (int i = 0; i < 4; ++i) gv[i] = LDB128(rws, (i == 3 && w >= 4) ? WS_END : WS_G + ((w + 8 * i) * 64 + lane) * 16, 0, AUX_SC1);
#pragma unroll
      for (int i = 0; i < 4; ++i) { const int ci = (w + 8 * i) * 64 + lane, row = ci / 224; if (i < 3 || w < 4) *(LAS u32x4*)(L + L_P + row * G_STR + (ci - row * 224) * 16) = gv[i]; }
    }
    wg_barrier();
    f32x4 acc = {0.f, 0.f, 0.f, 0.f};
#pragma unroll
    for (int u = 0; u < 7; ++u) acc = mfma32(*(LAS u32x4*)(L + L_P + (lane & 7) * G_STR + ((7 * w + u) * 32 + 8 * g) * 2), Bf[u], acc);
    if (lane < 32) {
#pragma unroll
      for (int r = 0; r < 4; ++r) *(LAS float*)(L + L_RED + ((w * 8 + 4 * g + r) * 16 + c16) * 4) = acc[r];
    }
    wg_barrier();
    if (w == 0) {
      f32x2 s = hres;
#pragma unroll
      for (int ww = 0; ww < 8; ++ww) s += *(LAS f32x2*)(L + L_RED + ((ww * 8 + rb) * 16 + 2 * cp) * 4);
      *(GAS u32*)((GAS unsigned char*)out + (rb * DM + 16 * cdn + 2 * cp) * 2) = cvtpk(s[0], s[1]);
    }
    STAMP(8);
  }
  STAMP_FLUSH();
}

extern "C" void kernel_launch(void* const* d_in, const int* in_sizes, int n_in, void* d_out, int out_size, void* d_ws, size_t ws_size, hipStream_t stream) {
  fused_decode_block<<<NWG, NTHR, 0, stream>>>((const u16*)d_in[0], (const u16*)d_in[1], (const u16*)d_in[2], (const u16*)d_in[3], (const u16*)d_in[4],
                                              (const u16*)d_in[6], (const u16*)d_in[7], (const u16*)d_in[8], (u16*)d_out, (unsigned char*)d_ws);
}
```

```cpp
#include <hip/hip_runtime.h>
#include <hip/hip_bf16.h>
#include <stdint.h>

typedef unsigned short u16;
typedef unsigned u32;
typedef unsigned long long u64;
typedef u32 u32x4 __attribute__((ext_vector_type(4)));
typedef u32 u32x2 __attribute__((ext_vector_type(2)));
typedef float f32x4 __attribute__((ext_vector_type(4)));
typedef float f32x2 __attribute__((ext_vector_type(2)));
typedef short s16x8 __attribute__((ext_vector_type(8)));
typedef short s16x4 __attribute__((ext_vector_type(4)));
typedef __bf16 bf16x8_t __attribute__((ext_vector_type(8)));
#define GAS __attribute__((address_space(1)))
#define LAS __attribute__((address_space(3)))
#define RLX __ATOMIC_RELAXED, __HIP_MEMORY_SCOPE_AGENT

#define QPOS 0
#define HOLD 1
#define PASSES 1
#define SLEEP0 8
#define QSLEEP 4
#define LATEW 1
#define VB 8
#define KB 8
#define WAUX 2
#define FDELAY 0
#define PSLEEP 1
#define NOQ 0
#define THROTTLE 1
#define B3 1
#define QWAIT 30
#define DEPTH 4
#define LAGW 24
#define WOEARLY 0
#define E1SLEEP 3
#define E2EARLY 1
#define E2SLEEP 0
#define E3EARLY 1
#define E3SLEEP 0
#define LSLEEP 1
#define EARLY 1
#define EARLY_DRAIN() do { if (FDELAY) __builtin_amdgcn_s_sleep(FDELAY); } while (0)
#define STAMPS 0

constexpr int NB = 8, DM = 512, HQ = 32, HKV = 8, SK = 8192, DH = 128, FF = 1792;
constexpr int NWG = 256, NTHR = 512;
constexpr int NGU = FF / 32;
constexpr int TOK_WG = SK / 4;
#define GUD 4
static_assert(GUD % 4 == 0 && GUD <= 8, "every wave has 30..33 tiles");
constexpr float SC_LOG2 = 0.08838834764831845f * 1.4426950408889634f;
constexpr float LN_EPS = 1e-5f;
constexpr int AUX_NT = 2, AUX_SC1 = 16;
constexpr int SPIN_MAX = 1 << 16;

constexpr int FS = 256;
constexpr int WS_E    = 0;
constexpr int WS_H1F  = 4096;
constexpr int WS_H1E  = WS_H1F + 256 * FS;
constexpr int WS_H2F  = WS_H1E + 256 * FS;
constexpr int WS_H3F  = WS_H2F + 512 * FS;
constexpr int WS_QG   = WS_H3F + 512 * FS;
constexpr int WS_PART = WS_QG + HKV * NB * 4 * 512 * 8;
constexpr int WS_Y    = WS_PART + HKV * 32 * 4 * 128 * 2;
constexpr int WS_G    = WS_Y + HKV * NB * DM * 2;
constexpr int WS_END  = WS_G + NB * FF * 2;
constexpr int WS_STAMP = 2 << 20;

constexpr int L_ROLE = 0;
constexpr int L_WQ = 0;
constexpr int L_MRG = 65536;
constexpr int A_STR = 1040;
constexpr int L_A = L_MRG + 16384;
constexpr int L_RED = L_A + 8 * A_STR;
constexpr int L_Q = L_RED + 8192;
constexpr int L_ST = L_Q + 1024;
constexpr int L_CNT = L_ST + 192;
constexpr int L_LAM = L_ST + 256;
constexpr int P_STR = 1040;
constexpr int L_P = L_LAM + 512;
constexpr int G_STR = 3600;
constexpr int LDS_BYTES = L_P + 32 * P_STR;

typedef __bf16 bf16x2_t __attribute__((ext_vector_type(2)));
__device__ __forceinline__ u32 cvtpk(float lo, float hi) { const bf16x2_t r = {(__bf16)lo, (__bf16)hi}; return __builtin_bit_cast(u32, r); }
__device__ __forceinline__ float u2f(u32 v) { return __builtin_bit_cast(float, v); }
__device__ __forceinline__ u32 f2u(float v) { return __builtin_bit_cast(u32, v); }
__device__ __forceinline__ float bflo(u32 v) { return __builtin_bit_cast(float, v << 16); }
__device__ __forceinline__ float bfhi(u32 v) { return __builtin_bit_cast(float, v & 0xffff0000u); }
template <int CTRL> __device__ __forceinline__ float dpp(float x) { return __builtin_bit_cast(float, __builtin_amdgcn_mov_dpp(__builtin_bit_cast(int, x), CTRL, 0xf, 0xf, true)); }
__device__ __forceinline__ float xrow_max(float x) {
  auto s = __builtin_amdgcn_permlane16_swap(f2u(x), f2u(x), false, false);
  x = fmaxf(u2f(s[0]), u2f(s[1]));
  auto t = __builtin_amdgcn_permlane32_swap(f2u(x), f2u(x), false, false);
  return fmaxf(u2f(t[0]), u2f(t[1]));
}
__device__ __forceinline__ float xrow_sum(float x) {
  auto s = __builtin_amdgcn_permlane16_swap(f2u(x), f2u(x), false, false);
  x = u2f(s[0]) + u2f(s[1]);
  auto t = __builtin_amdgcn_permlane32_swap(f2u(x), f2u(x), false, false);
  return u2f(t[0]) + u2f(t[1]);
}
__device__ __forceinline__ float wave_sum(float x) {
  x += dpp<0xB1>(x); x += dpp<0x4E>(x); x += dpp<0x141>(x); x += dpp<0x140>(x);
  return xrow_sum(x);
}
__device__ __forceinline__ f32x4 mfma32(u32x4 a, u32x4 b, f32x4 c) {
  return __builtin_amdgcn_mfma_f32_16x16x32_bf16(__builtin_bit_cast(bf16x8_t, a), __builtin_bit_cast(bf16x8_t, b), c, 0, 0, 0);
}
__device__ __forceinline__ f32x4 mfma16(u32x2 a, u32x2 b, f32x4 c) {
  return __builtin_amdgcn_mfma_f32_16x16x16bf16_1k(__builtin_bit_cast(s16x4, a), __builtin_bit_cast(s16x4, b), c, 0, 0, 0);
}
typedef short v4i16_t __attribute__((ext_vector_type(4)));
__device__ __forceinline__ u32x2 tr_read(LAS unsigned char* p) {
  return __builtin_bit_cast(u32x2, __builtin_amdgcn_ds_read_tr16_b64_v4i16((LAS v4i16_t*)p));
}
__device__ __forceinline__ u32x4 bfrag(LAS unsigned char* p, int second_off) {
  const u32x2 a = tr_read(p), b = tr_read(p + second_off);
  return u32x4{a.x, a.y, b.x, b.y};
}
__device__ __forceinline__ __amdgpu_buffer_rsrc_t mkrs(const void* p, u32 bytes) { return __builtin_amdgcn_make_buffer_rsrc((void*)p, 0, bytes, 0x00020000); }
template <int V> struct ic { static constexpr int value = V; };
#define LDB128(rs, vo, so, aux) __builtin_amdgcn_raw_buffer_load_b128(rs, vo, so, aux)
constexpr u32 SENT = 0xFFFFFFFFu;
__device__ __forceinline__ u32 nosent(u32 d) { return d == SENT ? 0x7FFF7FFFu : d; }
__device__ __forceinline__ u32 umax(u32 a, u32 b) { return a > b ? a : b; }
__device__ __forceinline__ u32 mx4(u32 m, u32x4 v) { return umax(umax(umax(m, v.x), v.y), umax(v.z, v.w)); }
#define SB() __builtin_amdgcn_sched_barrier(0)
#define DRAIN() asm volatile("s_waitcnt vmcnt(0)" ::: "memory")
#define MEMFENCE() asm volatile("" ::: "memory")
#define RAWBAR() asm volatile("s_barrier" ::: "memory")
__device__ __forceinline__ void wg_barrier() { asm volatile("s_waitcnt lgkmcnt(0)\n\ts_barrier" ::: "memory"); }

__device__ __forceinline__ u32x4 ln_row(const float (&v)[8]) {
  const float piv = u2f(__builtin_amdgcn_readfirstlane(f2u(v[0])));
  float d[8], s = 0.f, q = 0.f;
#pragma unroll
  for (int i = 0; i < 8; ++i) { d[i] = v[i] - piv; s += d[i]; q = fmaf(d[i], d[i], q); }
  s += dpp<0xB1>(s); q += dpp<0xB1>(q); s += dpp<0x4E>(s); q += dpp<0x4E>(q); s += dpp<0x141>(s); q += dpp<0x141>(q); s += dpp<0x140>(s); q += dpp<0x140>(q);
  s = xrow_sum(s); q = xrow_sum(q);
  const float mean = s * (1.0f / DM);
  const float var = fmaxf(fmaf(-mean, mean, q * (1.0f / DM)), 0.f);
  const float rs = __builtin_amdgcn_rsqf(var + LN_EPS), off = -mean * rs;
  return u32x4{cvtpk(fmaf(d[0], rs, off), fmaf(d[1], rs, off)), cvtpk(fmaf(d[2], rs, off), fmaf(d[3], rs, off)), cvtpk(fmaf(d[4], rs, off), fmaf(d[5], rs, off)), cvtpk(fmaf(d[6], rs, off), fmaf(d[7], rs, off))};
}

#define STAMP(i) do {} while (0)
#define RAWSTAMP(i, v) do {} while (0)
#define TSTAMP(i) do {} while (0)
#define STAMP_FLUSH() do {} while (0)

__global__ void __launch_bounds__(NTHR, 2)
fused_decode_block(const u16* x_, const u16* Kc_, const u16* Vc_, const u16* wq_, const u16* wo_,
                   const u16* wg_, const u16* wu_, const u16* wd_, u16* out_, unsigned char* ws_) {
  const u16 *x = x_, *Kc = Kc_, *Vc = Vc_, *wq = wq_, *wo = wo_, *wg = wg_, *wu = wu_, *wd = wd_; u16* out = out_; unsigned char* ws = ws_;
  asm volatile("" : "+s"(x), "+s"(Kc), "+s"(Vc), "+s"(wq), "+s"(wo), "+s"(wg), "+s"(wu), "+s"(wd), "+s"(out), "+s"(ws));
  __shared__ __attribute__((aligned(16))) unsigned char lds_raw[LDS_BYTES];
  LAS unsigned char* L = (LAS unsigned char*)lds_raw;
  const int tid = threadIdx.x, lane = tid & 63, w = __builtin_amdgcn_readfirstlane(tid >> 6);
  const int blk = blockIdx.x, kvh = blk & 7, j = blk >> 3, b = j >> 2, sp = j & 3;
  const int g = lane >> 4, c16 = lane & 15;
  GAS unsigned char* wsg = (GAS unsigned char*)ws;
  STAMP(0);

  u64 E;
  asm volatile("s_load_dwordx2 %0, %1, 0x0" : "=s"(E) : "s"(ws));
  const __amdgpu_buffer_rsrc_t rx = mkrs(x, NB * DM * 2);
  const __amdgpu_buffer_rsrc_t rwq = mkrs(wq, DM * HQ * DH * 2);
  const int xt = b < 7 ? 130 + GUD / 4 : 130;
  const int nt0 = xt - 8, nt1 = b < 7 ? xt - GUD : xt;
  const int ntile = sp == 0 ? nt0 : (sp == 1 ? nt1 : xt), tile0 = sp == 0 ? 0 : (sp == 1 ? nt0 : nt0 + nt1 + (sp - 2) * xt);
  const size_t kvbase = ((size_t)(b * HKV + kvh) * SK + (size_t)tile0 * 16) * DH;
  const __amdgpu_buffer_rsrc_t rk = mkrs(Kc + kvbase, ntile * 16 * DH * 2);
  const __amdgpu_buffer_rsrc_t rv = mkrs(Vc + kvbase, ntile * 16 * DH * 2);
  const __amdgpu_buffer_rsrc_t rws = mkrs(ws, WS_END);

  const int trl = (8 * g + ((lane >> 2) & 3)) * 32 + (lane & 3) * 8;
  const int role = sp == 0 ? 1 : sp == 1 ? (b < 7 ? 2 : 3) : (sp == 2 && b < 3 ? 3 : 0);
  const int cgu = kvh * 7 + b, cdn = kvh * 4 + (sp == 1 ? 0 : 1 + b);
#define LOAD_WEIGHTS(wst) do { \
      const u16* p0 = role == 1 ? wo : (role == 2 ? wg : wd); \
      const u16* p1 = role == 2 ? wu : p0; \
      const u32 n0b = role == 0 ? 0u : (role == 1 ? (u32)(HQ * DH * DM * 2) : (u32)(DM * FF * 2)); \
      const u32 n1b = role == 3 ? 0u : n0b; \
      const __amdgpu_buffer_rsrc_t r0 = mkrs(p0, n0b), r1 = mkrs(p1, n0b), r2 = mkrs(p1, n1b); \
      const int v0o = role == 1 ? ((kvh * 512 + 128 * w + (lane >> 3)) * DM + 64 * b + 8 * (lane & 7)) * 2 \
                    : role == 2 ? ((128 * w + (lane >> 2)) * FF + 32 * cgu + 8 * (lane & 3)) * 2 \
                                : ((448 * w + (lane >> 1)) * DM + 16 * cdn + 8 * (lane & 1)) * 2; \
      const int st = role == 1 ? 8 * DM * 2 : role == 2 ? 16 * FF * 2 : 32 * DM * 2; \
      const int v8o = role == 2 ? v0o : v0o + 8 * st; \
      _Pragma("unroll") for (int i = 0; i < 8; ++i) wst[i] = LDB128(r0, v0o + i * st, 0, WAUX); \
      _Pragma("unroll") for (int i = 0; i < 6; ++i) wst[8 + i] = LDB128(r1, v8o + i * st, 0, WAUX); \
      _Pragma("unroll") for (int i = 6; i < 8; ++i) wst[8 + i] = LDB128(r2, v8o + i * st, 0, WAUX); \
      SB(); } while (0)
#define STAGE_WEIGHTS(wst) do { \
    if (role == 1) { \
      _Pragma("unroll") for (int i = 0; i < 16; ++i) { const int r = 128 * w + 8 * i + (lane >> 3), ch = lane & 7; \
        *(LAS u32x4*)(L + L_ROLE + (r ^ ((r >> 2) & 1)) * 128 + 32 * ((ch >> 1) ^ (((r >> 3) & 1) | (((r >> 5) & 1) << 1))) + 16 * (ch & 1)) = wst[i]; } \
    } else if (role == 2) { \
      _Pragma("unroll") for (int i = 0; i < 16; ++i) { const int r = 128 * w + 16 * (i & 7) + (lane >> 2), ch = lane & 3; \
        *(LAS u32x4*)(L + L_ROLE + (i >> 3) * 32768 + r * 64 + 32 * ((ch >> 1) ^ ((r >> 3) & 1)) + 16 * (ch & 1)) = wst[i]; } \
    } else if (role == 3) { \
      _Pragma("unroll") for (int i = 0; i < 14; ++i) *(LAS u32x4*)(L + L_ROLE + (448 * w + 32 * i) * 32 + lane * 16) = wst[i]; \
    } } while (0)
  const int rb = lane >> 3, cp = lane & 7;
  u64 T64;
  if (w < 4) {
    const u32x4 xr0 = LDB128(rx, (2 * w * DM + 8 * lane) * 2, 0, 0), xr1 = LDB128(rx, ((2 * w + 1) * DM + 8 * lane) * 2, 0, 0);
    u32x4 wqr[4];
#pragma unroll
    for (int i = 0; i < 4; ++i) wqr[i] = LDB128(rwq, ((128 * sp + 32 * w + 8 * i + (lane >> 3)) * (HQ * DH) + kvh * 512 + 64 * b + 8 * (lane & 7)) * 2, 0, WAUX);
    SB(); RAWBAR(); SB();
    float v0[8], v1[8];
#pragma unroll
    for (int i = 0; i < 4; ++i) { v0[2 * i] = bflo(xr0[i]); v0[2 * i + 1] = bfhi(xr0[i]); v1[2 * i] = bflo(xr1[i]); v1[2 * i + 1] = bfhi(xr1[i]); }
    const u32x4 n0 = ln_row(v0), n1 = ln_row(v1);
    *(LAS u32x4*)(L + L_A + 2 * w * A_STR + lane * 16) = n0;
    *(LAS u32x4*)(L + L_A + (2 * w + 1) * A_STR + lane * 16) = n1;
#pragma unroll
    for (int i = 0; i < 4; ++i) *(LAS u32x4*)(L + L_WQ + (32 * w + 8 * i + (lane >> 3)) * 144 + (lane & 7) * 16) = wqr[i];
    STAMP(12);
    wg_barrier();
    STAMP(13);
    const u32x4 a = *(LAS u32x4*)(L + L_A + (lane & 7) * A_STR + (128 * sp + 32 * w + 8 * g) * 2);
#pragma unroll
    for (int nt = 0; nt < 4; ++nt) {
      LAS unsigned char* p = L + L_WQ + (32 * w + 8 * g + ((lane >> 2) & 3)) * 144 + nt * 32 + (lane & 3) * 8;
      const u32x2 lo = tr_read(p), hi = tr_read(p + 4 * 144);
      const f32x4 acc = mfma32(a, u32x4{lo.x, lo.y, hi.x, hi.y}, f32x4{0.f, 0.f, 0.f, 0.f});
      if (lane < 32) {
#pragma unroll
        for (int r = 0; r < 4; ++r) *(LAS float*)(L + L_P + ((w * 8 + 4 * g + r) * 64 + 16 * nt + c16) * 4) = acc[r];
      }
    }
    wg_barrier();
    STAMP(14);
    asm volatile("s_waitcnt lgkmcnt(0)" : "+s"(E));
    T64 = E + 1;
    const u64 T40 = T64 & 0xFFFFFFFFFFull;
#pragma unroll
    for (int i = 0; i < 2; ++i) {
      const int o = tid + 256 * i;
      float sum = 0.f;
#pragma unroll
      for (int ww = 0; ww < 4; ++ww) sum += *(LAS float*)(L + L_P + (ww * 512 + o) * 4);
      const u32 sv = f2u(sum) + 0x80u;
      __hip_atomic_store((GAS u64*)(wsg + WS_QG) + ((kvh * 8 + (o >> 6)) * 4 + sp) * 512 + 64 * b + (o & 63), (T40 << 24) | (u64)(sv >> 8), RLX);
    }
    STAMP(1);
    SB(); RAWBAR(); SB();
    __builtin_amdgcn_s_sleep(QWAIT);
    {
      GAS u64* qgp = (GAS u64*)(wsg + WS_QG) + (kvh * 8 + b) * 4 * 512 + tid;
      u64 qv[8]; int spins = 0;
      for (;;) {
        bool ok = true;
#pragma unroll
        for (int i = 0; i < 8; ++i) qv[i] = __hip_atomic_load(qgp + (i & 3) * 512 + (i >> 2) * 256, RLX);
#pragma unroll
        for (int i = 0; i < 8; ++i) ok &= (qv[i] >> 24) == T40;
        if (__all(ok) || ++spins > SPIN_MAX) break;
        MEMFENCE(); __builtin_amdgcn_s_sleep(2);
      }
      const float qa = (u2f((u32)qv[0] << 8) + u2f((u32)qv[1] << 8)) + (u2f((u32)qv[2] << 8) + u2f((u32)qv[3] << 8));
      const float qb = (u2f((u32)qv[4] << 8) + u2f((u32)qv[5] << 8)) + (u2f((u32)qv[6] << 8) + u2f((u32)qv[7] << 8));
      *(LAS u16*)(L + L_Q + tid * 2) = (u16)cvtpk(qa, qa);
      *(LAS u16*)(L + L_Q + 512 + tid * 2) = (u16)cvtpk(qb, qb);
    }
    STAMP(15);
    wg_barrier();
    SB();
    if (w == 3) {
      const u32x4 sv = {SENT, SENT, SENT, SENT};
      __builtin_amdgcn_raw_buffer_store_b128(sv, rws, WS_PART + (kvh * 32 + j) * 1024 + lane * 16, 0, AUX_SC1);
      if (role == 1) __builtin_amdgcn_raw_buffer_store_b128(sv, rws, WS_Y + ((kvh * 8 + rb) * DM + 64 * b + 8 * cp) * 2, 0, AUX_SC1);
      if (role == 2) __builtin_amdgcn_raw_buffer_store_b64(u32x2{SENT, SENT}, rws, WS_G + (rb * FF + 32 * cgu + 4 * cp) * 2, 0, AUX_SC1);
    }
    if (role == 1 || !LATEW) { u32x4 wst[16]; LOAD_WEIGHTS(wst); STAGE_WEIGHTS(wst); }
    DRAIN();
    wg_barrier();
  } else {
    const int sw = w - 4;
    const int koff = (4 * (c16 & 3) + (c16 >> 2)) * 256 + g * 16, voff = lane * 16;
    const bool vkeep = b >= VB;
#define ISSUE_K(slot, it) do { const int so_ = ((it) * 4 + sw) * 4096; \
    _Pragma("unroll") for (int u = 0; u < 4; ++u) Kb[slot][u] = LDB128(rk, koff + u * 64, so_, KAUX); SB(); } while (0)
#define ISSUE_V(slot, it) do { const int so_ = ((it) * 4 + sw) * 4096; \
    _Pragma("unroll") for (int u = 0; u < 4; ++u) Vb[slot][u] = LDB128(rv, voff + u * 1024, so_, VAUX); SB(); } while (0)
#define ISSUE(slot, it) do { ISSUE_K(slot, it); ISSUE_V(slot, it); } while (0)
    float mrun = -INFINITY, lrun = 0.f;
    f32x4 O1 = {0.f, 0.f, 0.f, 0.f}, O2 = {0.f, 0.f, 0.f, 0.f};
    auto stream = [&](auto kaux, auto vaux) __attribute__((always_inline)) {
    constexpr int KAUX = decltype(kaux)::value, VAUX = decltype(vaux)::value;
    u32x4 Kb[DEPTH][4], Vb[DEPTH][4];
    RAWBAR();
    if (SLEEP0) __builtin_amdgcn_s_sleep(SLEEP0);
    SB();
    ISSUE(0, 0);
    TSTAMP(12);
    RAWBAR(); SB();
    TSTAMP(13);
    ISSUE_V(1, 1);
    RAWBAR(); SB();
    RAWBAR(); SB();
    ISSUE_K(1, 1);
    asm volatile("s_waitcnt vmcnt(%0)" :: "n"(LAGW) : "memory"); SB();
    TSTAMP(14);
    ISSUE_K(2, 2);
    asm volatile("s_waitcnt vmcnt(%0)" :: "n"(LAGW) : "memory"); SB();
    ISSUE_V(2, 2);
    asm volatile("s_waitcnt vmcnt(%0)" :: "n"(LAGW) : "memory"); SB();
    ISSUE_K(3, 3);
    asm volatile("s_waitcnt vmcnt(%0)" :: "n"(LAGW) : "memory"); SB();
    ISSUE_V(3, 3);
    TSTAMP(15);
    wg_barrier();
    STAMP(2);
    u32x4 Qf[4];
#pragma unroll
    for (int u = 0; u < 4; ++u) Qf[u] = *(LAS u32x4*)(L + L_Q + ((lane & 3) * 128 + 32 * u + 8 * g) * 2);
    const int jx = c16 >> 2;
    const u32 m0k = jx == 0 ? 0x0000FFFFu : jx == 1 ? 0xFFFF0000u : 0u;
    const u32 m1k = jx == 2 ? 0x0000FFFFu : jx == 3 ? 0xFFFF0000u : 0u;
#define PROCESS(slot) do { \
    f32x4 s_ = {0.f, 0.f, 0.f, 0.f}; \
    _Pragma("unroll") for (int u = 0; u < 4; ++u) s_ = mfma32(Kb[slot][u], Qf[u], s_); \
    const float tm_ = xrow_max(fmaxf(fmaxf(s_[0], s_[1]), fmaxf(s_[2], s_[3]))); \
    if (__any(tm_ > mrun)) { \
      const float mn_ = fmaxf(mrun, tm_), al_ = __builtin_amdgcn_exp2f((mrun - mn_) * SC_LOG2); \
      lrun *= al_; mrun = mn_; \
      const float a0_ = dpp<0x00>(al_), a1_ = dpp<0x55>(al_), a2_ = dpp<0xAA>(al_), a3_ = dpp<0xFF>(al_); \
      O1[0] *= a0_; O1[1] *= a1_; O1[2] *= a2_; O1[3] *= a3_; O2[0] *= a0_; O2[1] *= a1_; O2[2] *= a2_; O2[3] *= a3_; \
    } \
    const float mc_ = mrun * SC_LOG2; \
    _Pragma("unroll") for (int i = 0; i < 4; ++i) { \
      const float p_ = __builtin_amdgcn_exp2f(fmaf(s_[i], SC_LOG2, -mc_)); lrun += p_; \
      const u32 pv_ = cvtpk(p_, p_); const u32x2 a_ = {pv_ & m0k, pv_ & m1k}; \
      O1 = mfma16(a_, u32x2{Vb[slot][i].x, Vb[slot][i].y}, O1); O2 = mfma16(a_, u32x2{Vb[slot][i].z, Vb[slot][i].w}, O2); \
    } SB(); } while (0)
#pragma unroll 1
    for (int it = 0; it < 28; it += 4) { PROCESS(0); ISSUE(0, it + 4); PROCESS(1); ISSUE(1, it + 5); PROCESS(2); ISSUE(2, it + 6); PROCESS(3); ISSUE(3, it + 7); }
    PROCESS(0); ISSUE(0, 32); PROCESS(1);
    if (30 * 4 + sw < ntile) PROCESS(2);
    if (31 * 4 + sw < ntile) PROCESS(3);
    if (32 * 4 + sw < ntile) PROCESS(0);
    };
    if (vkeep) stream(ic<0>{}, ic<0>{}); else stream(ic<AUX_NT>{}, ic<AUX_NT>{});
    asm volatile("s_waitcnt lgkmcnt(0)" : "+s"(E));
    T64 = E + 1;
    STAMP(3);
    lrun = xrow_sum(lrun);
#pragma unroll
    for (int h = 0; h < 4; ++h) *(LAS f32x2*)(L + L_MRG + ((sw * 4 + h) * 64 + lane) * 8) = f32x2{O1[h], O2[h]};
    if (lane < 4) { *(LAS float*)(L + L_ST + (lane * 4 + sw) * 4) = mrun; *(LAS float*)(L + L_ST + 64 + (lane * 4 + sw) * 4) = lrun; }
    wg_barrier();
  }
  const u32 T32 = (u32)T64;

  if (w == 1) { if (E1SLEEP) __builtin_amdgcn_s_sleep(E1SLEEP); if (lane == 0) __hip_atomic_store((GAS u64*)(wsg + WS_H1E + (kvh * 32 + j) * FS), T64, RLX); }
  if (w == 0) {
    const int h = g;
    const f32x4 ma = *(LAS f32x4*)(L + L_ST + h * 16), la = *(LAS f32x4*)(L + L_ST + 64 + h * 16);
    const float M = fmaxf(fmaxf(ma[0], ma[1]), fmaxf(ma[2], ma[3]));
    float Ls = 0.f;
    f32x4 oa = {0.f, 0.f, 0.f, 0.f}, ob = {0.f, 0.f, 0.f, 0.f};
#pragma unroll
    for (int ww = 0; ww < 4; ++ww) {
      const float f = __builtin_amdgcn_exp2f((ma[ww] - M) * SC_LOG2);
      Ls = fmaf(f, la[ww], Ls);
      oa += f * *(LAS f32x4*)(L + L_MRG + ((ww * 4 + h) * 64 + 4 * c16) * 8);
      ob += f * *(LAS f32x4*)(L + L_MRG + ((ww * 4 + h) * 64 + 4 * c16) * 8 + 16);
    }
    const float inv = __builtin_amdgcn_rcpf(Ls);
    const u32x4 pk = {nosent(cvtpk(oa[0] * inv, oa[1] * inv)), nosent(cvtpk(oa[2] * inv, oa[3] * inv)), nosent(cvtpk(ob[0] * inv, ob[1] * inv)), nosent(cvtpk(ob[2] * inv, ob[3] * inv))};
    __builtin_amdgcn_raw_buffer_store_b128(pk, rws, WS_PART + (kvh * 32 + j) * 1024 + lane * 16, 0, AUX_SC1);
    const float lam = fmaf(M, SC_LOG2, __builtin_amdgcn_logf(Ls));
    EARLY_DRAIN();
    if (c16 == 0) __hip_atomic_store((GAS u64*)(wsg + WS_H1F + (kvh * 32 + j) * FS) + h, ((u64)T32 << 32) | f2u(lam), RLX);
  }
  STAMP(4);
  if (LATEW && role >= 2) {
    if (tid == 0) *(LAS u32*)(L + L_CNT + 8) = 0u;
    SB(); RAWBAR(); SB();
    if (w < 4) { u32x4 wst[16]; LOAD_WEIGHTS(wst); STAGE_WEIGHTS(wst); }
    wg_barrier();
  }

  if (role == 1) {
    const int h = w >> 1, np = w & 1;
    u32x4 Bf[2][4];
    {
      const int q = (lane >> 2) & 3;
#pragma unroll
      for (int n = 0; n < 2; ++n)
#pragma unroll
        for (int u = 0; u < 4; ++u) {
          const int d0 = h * 128 + 32 * g + 8 * (q >> 1) + 4 * (q & 1) + u, d1 = d0 + 16;
          const int sg = (q >> 1) | ((g & 1) << 1);
          const u32x2 lo = tr_read(L + L_ROLE + (d0 ^ (q & 1)) * 128 + 32 * ((2 * np + n) ^ sg) + (lane & 3) * 8);
          const u32x2 hi = tr_read(L + L_ROLE + (d1 ^ (q & 1)) * 128 + 32 * ((2 * np + n) ^ sg) + (lane & 3) * 8);
          Bf[n][u] = u32x4{lo.x, lo.y, hi.x, hi.y};
        }
    }
    GAS u64* fp = (GAS u64*)(wsg + WS_H1F + (kvh * 32 + 4 * w) * FS) + g;
    const int po = WS_PART + (kvh * 32 + 4 * w) * 1024 + lane * 16;
    LAS unsigned char* pl = L + L_P + w * P_STR + lane * 16;
    LAS unsigned char* al = L + L_P + (lane & 7) * P_STR + h * 256 + g * 16;
    LAS unsigned char* rl = L + L_RED + ((h * 8 + 4 * g) * 64 + 32 * np + c16) * 4;
    int spins = 0;
    {
      GAS u64* ep = (GAS u64*)(wsg + WS_H1E + (kvh * 32 + 4 * w + (lane & 3)) * FS);
      for (;;) {
        if (__all(__hip_atomic_load(ep, RLX) == T64) || ++spins > SPIN_MAX) break;
        MEMFENCE(); __builtin_amdgcn_s_sleep(1);
      }
    }
    MEMFENCE();
    TSTAMP(12);
    spins = 0;
    for (;;) {
      const u64 f0 = __hip_atomic_load(fp, RLX), f1 = __hip_atomic_load(fp + FS / 8, RLX), f2 = __hip_atomic_load(fp + 2 * FS / 8, RLX), f3 = __hip_atomic_load(fp + 3 * FS / 8, RLX);
      u32x4 pv[4];
#pragma unroll
      for (int i = 0; i < 4; ++i) pv[i] = LDB128(rws, po + i * 1024, 0, AUX_SC1);
      SB();
      const float l0 = u2f((u32)f0), l1 = u2f((u32)f1), l2 = u2f((u32)f2), l3 = u2f((u32)f3);
      const float lm = fmaxf(fmaxf(l0, l1), fmaxf(l2, l3));
      float e[4] = {__builtin_amdgcn_exp2f(l0 - lm), __builtin_amdgcn_exp2f(l1 - lm), __builtin_amdgcn_exp2f(l2 - lm), __builtin_amdgcn_exp2f(l3 - lm)};
      const float inv = __builtin_amdgcn_rcpf((e[0] + e[1]) + (e[2] + e[3]));
#pragma unroll
      for (int i = 0; i < 4; ++i) e[i] *= inv;
      SB();
      u32 m = 0; f32x2 o[4];
#pragma unroll
      for (int d = 0; d < 4; ++d) o[d] = f32x2{0.f, 0.f};
#pragma unroll
      for (int i = 0; i < 4; ++i) {
        m = mx4(m, pv[i]);
#pragma unroll
        for (int d = 0; d < 4; ++d) o[d] += e[i] * f32x2{bflo(pv[i][d]), bfhi(pv[i][d])};
      }
      *(LAS u32x4*)pl = u32x4{cvtpk(o[0][0], o[0][1]), cvtpk(o[1][0], o[1][1]), cvtpk(o[2][0], o[2][1]), cvtpk(o[3][0], o[3][1])};
      const bool ok = (m != SENT) & ((u32)(f0 >> 32) == T32) & ((u32)(f1 >> 32) == T32) & ((u32)(f2 >> 32) == T32) & ((u32)(f3 >> 32) == T32);
      if (__all(ok) || ++spins > SPIN_MAX) break;
      MEMFENCE();
    }
    RAWSTAMP(10, spins + 1);
    TSTAMP(13);
    wg_barrier();
    if (w == 1 && E2EARLY) { if (E2SLEEP) __builtin_amdgcn_s_sleep(E2SLEEP); if (lane < 8) __hip_atomic_store((GAS u64*)(wsg + WS_H2F + (lane * 64 + kvh * 8 + b) * FS), T64, RLX); }
    TSTAMP(14);
    {
      u32x4 av[4];
#pragma unroll
      for (int u = 0; u < 4; ++u) av[u] = *(LAS u32x4*)(al + u * 64);
      SB();
      f32x4 a0 = {0.f, 0.f, 0.f, 0.f}, a1 = {0.f, 0.f, 0.f, 0.f};
#pragma unroll
      for (int u = 0; u < 4; ++u) { a0 = mfma32(av[u], Bf[0][u], a0); a1 = mfma32(av[u], Bf[1][u], a1); }
      if (lane < 32) {
#pragma unroll
        for (int r = 0; r < 4; ++r) { *(LAS float*)(rl + r * 256) = a0[r]; *(LAS float*)(rl + r * 256 + 64) = a1[r]; }
      }
    }
    TSTAMP(15);
    wg_barrier();
    STAMP(5);
    if (w == 0) {
      f32x4 ra[4], rc[4];
#pragma unroll
      for (int hh = 0; hh < 4; ++hh) { ra[hh] = *(LAS f32x4*)(L + L_RED + ((hh * 8 + rb) * 64 + 8 * cp) * 4); rc[hh] = *(LAS f32x4*)(L + L_RED + ((hh * 8 + rb) * 64 + 8 * cp) * 4 + 16); }
      const f32x4 sa = (ra[0] + ra[1]) + (ra[2] + ra[3]), sb = (rc[0] + rc[1]) + (rc[2] + rc[3]);
      const u32x4 pk = {nosent(cvtpk(sa[0], sa[1])), nosent(cvtpk(sa[2], sa[3])), nosent(cvtpk(sb[0], sb[1])), nosent(cvtpk(sb[2], sb[3]))};
      __builtin_amdgcn_raw_buffer_store_b128(pk, rws, WS_Y + ((kvh * 8 + rb) * DM + 64 * b + 8 * cp) * 2, 0, AUX_SC1);
      if (!E2EARLY && lane < 8) __hip_atomic_store((GAS u64*)(wsg + WS_H2F + (lane * 64 + kvh * 8 + b) * FS), T64, RLX);
    }
    STAMP(6);
  } else if (role == 2) {
    const int mat = w & 1, kq = w >> 1;
    u32x4 Bf[2][4];
    {
      const int q = (lane >> 2) & 3;
#pragma unroll
      for (int n = 0; n < 2; ++n)
#pragma unroll
        for (int u = 0; u < 4; ++u) {
          LAS unsigned char* p = L + L_ROLE + mat * 32768 + (128 * kq + 32 * u + 8 * g + q) * 64 + 32 * (n ^ (g & 1)) + (lane & 3) * 8;
          const u32x2 lo = tr_read(p), hi = tr_read(p + 256);
          Bf[n][u] = u32x4{lo.x, lo.y, hi.x, hi.y};
        }
    }
    const u32x4 xv = LDB128(rx, (w * DM + 8 * lane) * 2, 0, 0);
    float x8[8];
#pragma unroll
    for (int i = 0; i < 4; ++i) { x8[2 * i] = bflo(xv[i]); x8[2 * i + 1] = bfhi(xv[i]); }
    const int yo = WS_Y + (w * DM + 8 * lane) * 2;
    LAS unsigned char* aw = L + L_A + w * A_STR + lane * 16;
    LAS unsigned char* ar = L + L_A + (lane & 7) * A_STR + (128 * kq + 8 * g) * 2;
    LAS unsigned char* rw = L + L_RED + (((kq * 2 + mat) * 8 + 4 * g) * 32 + c16) * 4;
    u32x4 yv[8];
    {
      GAS u64* fp = (GAS u64*)(wsg + WS_H2F + (kvh * 64 + lane) * FS);
      LAS u32* mk = (LAS u32*)(L + L_CNT + 8);
      u32 done = 0; int spins = 0;
#pragma unroll
      for (int i = 0; i < 8; ++i) yv[i] = u32x4{0u, 0u, 0u, 0u};
      for (;;) {
        u32 rdy;
        if (w == 0) {
          const u64 bal = __builtin_amdgcn_ballot_w64(__hip_atomic_load(fp, RLX) == T64);
          rdy = 0;
#pragma unroll
          for (int i = 0; i < 8; ++i) rdy |= (((bal >> (8 * i)) & 0xFFull) == 0xFFull ? 1u : 0u) << i;
          if (rdy != done && lane == 0) __hip_atomic_store(mk, rdy, __ATOMIC_RELAXED, __HIP_MEMORY_SCOPE_WORKGROUP);
        } else {
          rdy = __builtin_amdgcn_readfirstlane(__hip_atomic_load(mk, __ATOMIC_RELAXED, __HIP_MEMORY_SCOPE_WORKGROUP));
        }
#pragma unroll
        for (int i = 0; i < 8; ++i)
          if (((rdy & ~done) >> i) & 1u) yv[i] = LDB128(rws, yo + i * (8 * DM * 2), 0, AUX_SC1);
        done |= rdy;
        if (done == 0xFFu || ++spins > SPIN_MAX * 8) break;
        MEMFENCE(); if (w == 0) { if (PSLEEP) __builtin_amdgcn_s_sleep(PSLEEP); } else __builtin_amdgcn_s_sleep(LSLEEP);
      }
    }
    STAMP(7);
    {
      int spins = 0;
      for (;;) {
        u32 bad = 0;
#pragma unroll
        for (int i = 0; i < 8; ++i) if (!__all(mx4(0u, yv[i]) != SENT)) bad |= 1u << i;
        if (bad == 0 || ++spins > SPIN_MAX) break;
        MEMFENCE();
#pragma unroll
        for (int i = 0; i < 8; ++i) if ((bad >> i) & 1u) yv[i] = LDB128(rws, yo + i * (8 * DM * 2), 0, AUX_SC1);
        RAWSTAMP(11, spins + 1);
      }
    }
    float v[8];
#pragma unroll
    for (int e = 0; e < 8; ++e) v[e] = x8[e];
#pragma unroll
    for (int i = 0; i < 8; ++i) {
#pragma unroll
      for (int e = 0; e < 4; ++e) { v[2 * e] += bflo(yv[i][e]); v[2 * e + 1] += bfhi(yv[i][e]); }
    }
    TSTAMP(13);
    *(LAS u32x4*)aw = ln_row(v);
    TSTAMP(14);
    wg_barrier();
    if (w == 1 && E3EARLY) { if (E3SLEEP) __builtin_amdgcn_s_sleep(E3SLEEP); if (lane < 8) __hip_atomic_store((GAS u64*)(wsg + WS_H3F + (lane * 64 + cgu) * FS), T64, RLX); }
    STAMP(8);
    {
      u32x4 av[4];
#pragma unroll
      for (int u = 0; u < 4; ++u) av[u] = *(LAS u32x4*)(ar + u * 64);
      SB();
      f32x4 a0 = {0.f, 0.f, 0.f, 0.f}, a1 = {0.f, 0.f, 0.f, 0.f};
#pragma unroll
      for (int u = 0; u < 4; ++u) { a0 = mfma32(av[u], Bf[0][u], a0); a1 = mfma32(av[u], Bf[1][u], a1); }
      if (lane < 32) {
#pragma unroll
        for (int r = 0; r < 4; ++r) { *(LAS float*)(rw + r * 128) = a0[r]; *(LAS float*)(rw + r * 128 + 64) = a1[r]; }
      }
    }
    TSTAMP(15);
    wg_barrier();
    if (w == 0) {
      f32x4 gq[4], uq[4];
#pragma unroll
      for (int q = 0; q < 4; ++q) { gq[q] = *(LAS f32x4*)(L + L_RED + (((q * 2 + 0) * 8 + rb) * 32 + 4 * cp) * 4); uq[q] = *(LAS f32x4*)(L + L_RED + (((q * 2 + 1) * 8 + rb) * 32 + 4 * cp) * 4); }
      const f32x4 ga = (gq[0] + gq[1]) + (gq[2] + gq[3]), up = (uq[0] + uq[1]) + (uq[2] + uq[3]);
      float r[4];
#pragma unroll
      for (int e = 0; e < 4; ++e) {
        const float z = ga[e];
        const float t = z * fmaf(z * z, 0.044715f * 0.7978845608028654f, 0.7978845608028654f);
        const float sg = __builtin_amdgcn_rcpf(1.0f + __builtin_amdgcn_exp2f(t * (-2.0f * 1.4426950408889634f)));
        r[e] = z * sg * up[e];
      }
      __builtin_amdgcn_raw_buffer_store_b64(u32x2{nosent(cvtpk(r[0], r[1])), nosent(cvtpk(r[2], r[3]))}, rws, WS_G + (rb * FF + 32 * cgu + 4 * cp) * 2, 0, AUX_SC1);
      if (!E3EARLY && lane < 8) __hip_atomic_store((GAS u64*)(wsg + WS_H3F + (lane * 64 + cgu) * FS), T64, RLX);
    }
    STAMP(9);
  } else if (role == 3) {
    u32x4 Bf[7];
#pragma unroll
    for (int u = 0; u < 7; ++u) Bf[u] = bfrag(L + L_ROLE + (224 * w + 32 * u) * 32 + trl, 128);
    int go[4]; LAS unsigned char* gl[4];
#pragma unroll
    for (int i = 0; i < 4; ++i) {
      const int ci = (w + 8 * i) * 64 + lane, row = ci / 224; const bool in = i < 3 || w < 4;
      go[i] = in ? WS_G + ci * 16 : WS_END;
      gl[i] = L + L_P + (in ? row * G_STR + (ci - row * 224) * 16 : 8 * G_STR + lane * 16);
    }
    LAS unsigned char* ar = L + L_P + (lane & 7) * G_STR + (7 * w * 32 + 8 * g) * 2;
    LAS unsigned char* rw = L + L_RED + ((w * 8 + 4 * g) * 16 + c16) * 4;
    f32x2 hres = {0.f, 0.f};
    if (w == 0) {
      const u32 xw = *(const GAS u32*)((const GAS unsigned char*)x + (rb * DM + 16 * cdn + 2 * cp) * 2);
      GAS u64* fp = (GAS u64*)(wsg + WS_H2F + (kvh * 64 + (lane & 7) * 8 + (cdn >> 2)) * FS); int spins = 0;
      for (;;) { const u64 f0 = __hip_atomic_load(fp, RLX); if (__all(f0 == T64) || ++spins > SPIN_MAX) break; MEMFENCE(); __builtin_amdgcn_s_sleep(2); }
      MEMFENCE();
      for (spins = 0;; ) {
        u32 yy[8], m = 0;
#pragma unroll
        for (int q = 0; q < 8; ++q) yy[q] = __builtin_amdgcn_raw_buffer_load_b32(rws, WS_Y + ((q * 8 + rb) * DM + 16 * cdn + 2 * cp) * 2, 0, AUX_SC1);
        hres = f32x2{bflo(xw), bfhi(xw)};
#pragma unroll
        for (int q = 0; q < 8; ++q) { m = umax(m, yy[q]); hres += f32x2{bflo(yy[q]), bfhi(yy[q])}; }
        if (__all(m != SENT) || ++spins > SPIN_MAX) break;
        MEMFENCE();
      }
      GAS u64* gp = (GAS u64*)(wsg + WS_H3F + (kvh * 64 + lane) * FS); spins = 0;
      for (;;) {
        const u64 f0 = lane < NGU ? __hip_atomic_load(gp, RLX) : T64;
        if (__all(f0 == T64) || ++spins > SPIN_MAX) break;
        MEMFENCE(); if (PSLEEP) __builtin_amdgcn_s_sleep(PSLEEP);
      }
      if (cdn == 0 && lane == 0) __hip_atomic_store((GAS u64*)(wsg + WS_E), T64, RLX);
    }
    wg_barrier();
    STAMP(9);
    for (int spins = 0;; ) {
      u32x4 gv[4];
#pragma unroll
      for (int i = 0; i < 4; ++i) gv[i] = LDB128(rws, go[i], 0, AUX_SC1);
      SB();
      u32 m = 0;
#pragma unroll
      for (int i = 0; i < 4; ++i) { m = mx4(m, gv[i]); *(LAS u32x4*)gl[i] = gv[i]; }
      if (__all(m != SENT) || ++spins > SPIN_MAX) break;
      RAWSTAMP(11, spins + 1);
      MEMFENCE();
    }
    TSTAMP(12);
    wg_barrier();
    TSTAMP(13);
    {
      u32x4 av[7];
#pragma unroll
      for (int u = 0; u < 7; ++u) av[u] = *(LAS u32x4*)(ar + u * 64);
      SB();
      f32x4 a0 = {0.f, 0.f, 0.f, 0.f}, a1 = {0.f, 0.f, 0.f, 0.f};
#pragma unroll
      for (int u = 0; u < 7; ++u) { if (u & 1) a1 = mfma32(av[u], Bf[u], a1); else a0 = mfma32(av[u], Bf[u], a0); }
      if (lane < 32) {
#pragma unroll
        for (int r = 0; r < 4; ++r) *(LAS float*)(rw + r * 64) = a0[r] + a1[r];
      }
    }
    TSTAMP(14);
    wg_barrier();
    TSTAMP(15);
    if (w == 0) {
      f32x2 rr[8];
#pragma unroll
      for (int ww = 0; ww < 8; ++ww) rr[ww] = *(LAS f32x2*)(L + L_RED + ((ww * 8 + rb) * 16 + 2 * cp) * 4);
      const f32x2 sm = hres + (((rr[0] + rr[1]) + (rr[2] + rr[3])) + ((rr[4] + rr[5]) + (rr[6] + rr[7])));
      *(GAS u32*)((GAS unsigned char*)out + (rb * DM + 16 * cdn + 2 * cp) * 2) = cvtpk(sm[0], sm[1]);
    }
    STAMP(8);
  }
  STAMP_FLUSH();
}

extern "C" void kernel_launch(void* const* d_in, const int* in_sizes, int n_in, void* d_out, int out_size, void* d_ws, size_t ws_size, hipStream_t stream) {
  fused_decode_block<<<NWG, NTHR, 0, stream>>>((const u16*)d_in[0], (const u16*)d_in[1], (const u16*)d_in[2], (const u16*)d_in[3], (const u16*)d_in[4],
                                              (const u16*)d_in[6], (const u16*)d_in[7], (const u16*)d_in[8], (u16*)d_out, (unsigned char*)d_ws);
}
```

```cpp
#include <hip/hip_runtime.h>
#include <hip/hip_bf16.h>
#include <stdint.h>

typedef unsigned short u16;
typedef unsigned u32;
typedef unsigned long long u64;
typedef u32 u32x4 __attribute__((ext_vector_type(4)));
typedef u32 u32x2 __attribute__((ext_vector_type(2)));
typedef float f32x4 __attribute__((ext_vector_type(4)));
typedef float f32x2 __attribute__((ext_vector_type(2)));
typedef short s16x8 __attribute__((ext_vector_type(8)));
typedef short s16x4 __attribute__((ext_vector_type(4)));
typedef __bf16 bf16x8_t __attribute__((ext_vector_type(8)));
#define GAS __attribute__((address_space(1)))
#define LAS __attribute__((address_space(3)))
#define RLX __ATOMIC_RELAXED, __HIP_MEMORY_SCOPE_AGENT

#define QPOS 0
#define HOLD 1
#define PASSES 1
#define SLEEP0 8
#define QSLEEP 4
#define LATEW 1
#define VB 8
#define KB 8
#define WAUX 2
#define FDELAY 0
#define PSLEEP 1
#define NOQ 0
#define THROTTLE 1
#define B3 1
#define QWAIT 30
#define DEPTH 4
#define LAGW 24
#define WOEARLY 0
#define E1SLEEP 4
#define E2EARLY 1
#define E2SLEEP 3
#define E3EARLY 1
#define E3SLEEP 3
#define LSLEEP 1
#define EARLY 1
#define EARLY_DRAIN() do { if (FDELAY) __builtin_amdgcn_s_sleep(FDELAY); } while (0)
#define STAMPS 0

constexpr int NB = 8, DM = 512, HQ = 32, HKV = 8, SK = 8192, DH = 128, FF = 1792;
constexpr int NWG = 256, NTHR = 512;
constexpr int NGU = FF / 32;
constexpr int TOK_WG = SK / 4;
#define GUD 4
static_assert(GUD % 4 == 0 && GUD <= 8, "every wave has 30..33 tiles");
constexpr float SC_LOG2 = 0.08838834764831845f * 1.4426950408889634f;
constexpr float LN_EPS = 1e-5f;
constexpr int AUX_NT = 2, AUX_SC1 = 16;
constexpr int SPIN_MAX = 1 << 16;

constexpr int FS = 256;
constexpr int WS_E    = 0;
constexpr int WS_H1F  = 4096;
constexpr int WS_H1E  = WS_H1F + 256 * FS;
constexpr int WS_H2F  = WS_H1E + 256 * FS;
constexpr int WS_H3F  = WS_H2F + 512 * FS;
constexpr int WS_QG   = WS_H3F + 512 * FS;
constexpr int WS_PART = WS_QG + HKV * NB * 4 * 512 * 8;
constexpr int WS_Y    = WS_PART + HKV * 32 * 4 * 128 * 2;
constexpr int WS_G    = WS_Y + HKV * NB * DM * 2;
constexpr int WS_END  = WS_G + NB * FF * 2;
constexpr int WS_STAMP = 2 << 20;

constexpr int L_ROLE = 0;
constexpr int L_WQ = 0;
constexpr int L_MRG = 65536;
constexpr int A_STR = 1040;
constexpr int L_A = L_MRG + 16384;
constexpr int L_RED = L_A + 8 * A_STR;
constexpr int L_Q = L_RED + 8192;
constexpr int L_ST = L_Q + 1024;
constexpr int L_CNT = L_ST + 192;
constexpr int L_LAM = L_ST + 256;
constexpr int P_STR = 1040;
constexpr int L_P = L_LAM + 512;
constexpr int G_STR = 3600;
constexpr int LDS_BYTES = L_P + 32 * P_STR;

typedef __bf16 bf16x2_t __attribute__((ext_vector_type(2)));
__device__ __forceinline__ u32 cvtpk(float lo, float hi) { const bf16x2_t r = {(__bf16)lo, (__bf16)hi}; return __builtin_bit_cast(u32, r); }
__device__ __forceinline__ float u2f(u32 v) { return __builtin_bit_cast(float, v); }
__device__ __forceinline__ u32 f2u(float v) { return __builtin_bit_cast(u32, v); }
__device__ __forceinline__ float bflo(u32 v) { return __builtin_bit_cast(float, v << 16); }
__device__ __forceinline__ float bfhi(u32 v) { return __builtin_bit_cast(float, v & 0xffff0000u); }
template <int CTRL> __device__ __forceinline__ float dpp(float x) { return __builtin_bit_cast(float, __builtin_amdgcn_mov_dpp(__builtin_bit_cast(int, x), CTRL, 0xf, 0xf, true)); }
__device__ __forceinline__ float xrow_max(float x) {
  auto s = __builtin_amdgcn_permlane16_swap(f2u(x), f2u(x), false, false);
  x = fmaxf(u2f(s[0]), u2f(s[1]));
  auto t = __builtin_amdgcn_permlane32_swap(f2u(x), f2u(x), false, false);
  return fmaxf(u2f(t[0]), u2f(t[1]));
}
__device__ __forceinline__ float xrow_sum(float x) {
  auto s = __builtin_amdgcn_permlane16_swap(f2u(x), f2u(x), false, false);
  x = u2f(s[0]) + u2f(s[1]);
  auto t = __builtin_amdgcn_permlane32_swap(f2u(x), f2u(x), false, false);
  return u2f(t[0]) + u2f(t[1]);
}
__device__ __forceinline__ float wave_sum(float x) {
  x += dpp<0xB1>(x); x += dpp<0x4E>(x); x += dpp<0x141>(x); x += dpp<0x140>(x);
  return xrow_sum(x);
}
__device__ __forceinline__ f32x4 mfma32(u32x4 a, u32x4 b, f32x4 c) {
  return __builtin_amdgcn_mfma_f32_16x16x32_bf16(__builtin_bit_cast(bf16x8_t, a), __builtin_bit_cast(bf16x8_t, b), c, 0, 0, 0);
}
__device__ __forceinline__ f32x4 mfma16(u32x2 a, u32x2 b, f32x4 c) {
  return __builtin_amdgcn_mfma_f32_16x16x16bf16_1k(__builtin_bit_cast(s16x4, a), __builtin_bit_cast(s16x4, b), c, 0, 0, 0);
}
typedef short v4i16_t __attribute__((ext_vector_type(4)));
__device__ __forceinline__ u32x2 tr_read(LAS unsigned char* p) {
  return __builtin_bit_cast(u32x2, __builtin_amdgcn_ds_read_tr16_b64_v4i16((LAS v4i16_t*)p));
}
__device__ __forceinline__ u32x4 bfrag(LAS unsigned char* p, int second_off) {
  const u32x2 a = tr_read(p), b = tr_read(p + second_off);
  return u32x4{a.x, a.y, b.x, b.y};
}
constexpr u32 SENT = 0xFFFFFFFFu;
__device__ __forceinline__ u32 nosent(u32 d) { return d == SENT ? 0x7FFF7FFFu : d; }
__device__ __forceinline__ u32 umax(u32 a, u32 b) { return a > b ? a : b; }
__device__ __forceinline__ u32 mx4(u32 m, u32x4 v) { return umax(umax(umax(m, v.x), v.y), umax(v.z, v.w)); }
__device__ __forceinline__ u32x2 pack_pairs(const float (&a)[4], bool odd) {
  const float n0 = dpp<0xB1>(odd ? a[0] : a[2]), n1 = dpp<0xB1>(odd ? a[1] : a[3]);
  const float o0 = odd ? a[2] : a[0], o1 = odd ? a[3] : a[1];
  return u32x2{nosent(cvtpk(odd ? n0 : o0, odd ? o0 : n0)), nosent(cvtpk(odd ? n1 : o1, odd ? o1 : n1))};
}
__device__ __forceinline__ __amdgpu_buffer_rsrc_t mkrs(const void* p, u32 bytes) { return __builtin_amdgcn_make_buffer_rsrc((void*)p, 0, bytes, 0x00020000); }
template <int V> struct ic { static constexpr int value = V; };
#define LDB128(rs, vo, so, aux) __builtin_amdgcn_raw_buffer_load_b128(rs, vo, so, aux)
#define SB() __builtin_amdgcn_sched_barrier(0)
#define DRAIN() asm volatile("s_waitcnt vmcnt(0)" ::: "memory")
#define MEMFENCE() asm volatile("" ::: "memory")
#define RAWBAR() asm volatile("s_barrier" ::: "memory")
__device__ __forceinline__ void wg_barrier() { asm volatile("s_waitcnt lgkmcnt(0)\n\ts_barrier" ::: "memory"); }

__device__ __forceinline__ u32x4 ln_row(const float (&v)[8]) {
  const float piv = u2f(__builtin_amdgcn_readfirstlane(f2u(v[0])));
  float d[8], s = 0.f, q = 0.f;
#pragma unroll
  for (int i = 0; i < 8; ++i) { d[i] = v[i] - piv; s += d[i]; q = fmaf(d[i], d[i], q); }
  s += dpp<0xB1>(s); q += dpp<0xB1>(q); s += dpp<0x4E>(s); q += dpp<0x4E>(q); s += dpp<0x141>(s); q += dpp<0x141>(q); s += dpp<0x140>(s); q += dpp<0x140>(q);
  s = xrow_sum(s); q = xrow_sum(q);
  const float mean = s * (1.0f / DM);
  const float var = fmaxf(fmaf(-mean, mean, q * (1.0f / DM)), 0.f);
  const float rs = __builtin_amdgcn_rsqf(var + LN_EPS), off = -mean * rs;
  return u32x4{cvtpk(fmaf(d[0], rs, off), fmaf(d[1], rs, off)), cvtpk(fmaf(d[2], rs, off), fmaf(d[3], rs, off)), cvtpk(fmaf(d[4], rs, off), fmaf(d[5], rs, off)), cvtpk(fmaf(d[6], rs, off), fmaf(d[7], rs, off))};
}

#define STAMP(i) do {} while (0)
#define RAWSTAMP(i, v) do {} while (0)
#define TSTAMP(i) do {} while (0)
#define STAMP_FLUSH() do {} while (0)

__global__ void __launch_bounds__(NTHR, 2)
fused_decode_block(const u16* x_, const u16* Kc_, const u16* Vc_, const u16* wq_, const u16* wo_,
                   const u16* wg_, const u16* wu_, const u16* wd_, u16* out_, unsigned char* ws_) {
  const u16 *x = x_, *Kc = Kc_, *Vc = Vc_, *wq = wq_, *wo = wo_, *wg = wg_, *wu = wu_, *wd = wd_; u16* out = out_; unsigned char* ws = ws_;
  asm volatile("" : "+s"(x), "+s"(Kc), "+s"(Vc), "+s"(wq), "+s"(wo), "+s"(wg), "+s"(wu), "+s"(wd), "+s"(out), "+s"(ws));
  __shared__ __attribute__((aligned(16))) unsigned char lds_raw[LDS_BYTES];
  LAS unsigned char* L = (LAS unsigned char*)lds_raw;
  const int tid = threadIdx.x, lane = tid & 63, w = __builtin_amdgcn_readfirstlane(tid >> 6);
  const int blk = blockIdx.x, kvh = blk & 7, j = blk >> 3, b = j >> 2, sp = j & 3;
  const int g = lane >> 4, c16 = lane & 15;
  GAS unsigned char* wsg = (GAS unsigned char*)ws;
  STAMP(0);

  u64 E;
  asm volatile("s_load_dwordx2 %0, %1, 0x0" : "=s"(E) : "s"(ws));
  const __amdgpu_buffer_rsrc_t rx = mkrs(x, NB * DM * 2);
  const __amdgpu_buffer_rsrc_t rwq = mkrs(wq, DM * HQ * DH * 2);
  const int xt = b < 7 ? 130 + GUD / 4 : 130;
  const int nt0 = xt - 8, nt1 = b < 7 ? xt - GUD : xt;
  const int ntile = sp == 0 ? nt0 : (sp == 1 ? nt1 : xt), tile0 = sp == 0 ? 0 : (sp == 1 ? nt0 : nt0 + nt1 + (sp - 2) * xt);
  const size_t kvbase = ((size_t)(b * HKV + kvh) * SK + (size_t)tile0 * 16) * DH;
  const __amdgpu_buffer_rsrc_t rk = mkrs(Kc + kvbase, ntile * 16 * DH * 2);
  const __amdgpu_buffer_rsrc_t rv = mkrs(Vc + kvbase, ntile * 16 * DH * 2);
  const __amdgpu_buffer_rsrc_t rws = mkrs(ws, WS_END);

  const int trl = (8 * g + ((lane >> 2) & 3)) * 32 + (lane & 3) * 8;
  const int role = sp == 0 ? 1 : sp == 1 ? (b < 7 ? 2 : 3) : (sp == 2 && b < 3 ? 3 : 0);
  const int cgu = kvh * 7 + b, cdn = kvh * 4 + (sp == 1 ? 0 : 1 + b);
#define LOAD_WEIGHTS(wst) do { \
      const u16* p0 = role == 1 ? wo : (role == 2 ? wg : wd); \
      const u16* p1 = role == 2 ? wu : p0; \
      const u32 n0b = role == 0 ? 0u : (role == 1 ? (u32)(HQ * DH * DM * 2) : (u32)(DM * FF * 2)); \
      const u32 n1b = role == 3 ? 0u : n0b; \
      const __amdgpu_buffer_rsrc_t r0 = mkrs(p0, n0b), r1 = mkrs(p1, n0b), r2 = mkrs(p1, n1b); \
      const int v0o = role == 1 ? ((kvh * 512 + 128 * w + (lane >> 3)) * DM + 64 * b + 8 * (lane & 7)) * 2 \
                    : role == 2 ? ((128 * w + (lane >> 2)) * FF + 32 * cgu + 8 * (lane & 3)) * 2 \
                                : ((448 * w + (lane >> 1)) * DM + 16 * cdn + 8 * (lane & 1)) * 2; \
      const int st = role == 1 ? 8 * DM * 2 : role == 2 ? 16 * FF * 2 : 32 * DM * 2; \
      const int v8o = role == 2 ? v0o : v0o + 8 * st; \
      _Pragma("unroll") for (int i = 0; i < 8; ++i) wst[i] = LDB128(r0, v0o + i * st, 0, WAUX); \
      _Pragma("unroll") for (int i = 0; i < 6; ++i) wst[8 + i] = LDB128(r1, v8o + i * st, 0, WAUX); \
      _Pragma("unroll") for (int i = 6; i < 8; ++i) wst[8 + i] = LDB128(r2, v8o + i * st, 0, WAUX); \
      SB(); } while (0)
#define STAGE_WEIGHTS(wst) do { \
    if (role == 1) { \
      _Pragma("unroll") for (int i = 0; i < 16; ++i) { const int r = 128 * w + 8 * i + (lane >> 3), ch = lane & 7; \
        *(LAS u32x4*)(L + L_ROLE + (r ^ ((r >> 2) & 1)) * 128 + 32 * ((ch >> 1) ^ (((r >> 3) & 1) | (((r >> 5) & 1) << 1))) + 16 * (ch & 1)) = wst[i]; } \
    } else if (role == 2) { \
      _Pragma("unroll") for (int i = 0; i < 16; ++i) { const int r = 128 * w + 16 * (i & 7) + (lane >> 2), ch = lane & 3; \
        *(LAS u32x4*)(L + L_ROLE + (i >> 3) * 32768 + r * 64 + 32 * ((ch >> 1) ^ ((r >> 3) & 1)) + 16 * (ch & 1)) = wst[i]; } \
    } else if (role == 3) { \
      _Pragma("unroll") for (int i = 0; i < 14; ++i) *(LAS u32x4*)(L + L_ROLE + (448 * w + 32 * i) * 32 + lane * 16) = wst[i]; \
    } } while (0)
  const int rb = lane >> 3, cp = lane & 7;
  u64 T64;
  if (__builtin_expect(w < 4, 1)) {
    const u32x4 xr0 = LDB128(rx, (2 * w * DM + 8 * lane) * 2, 0, 0), xr1 = LDB128(rx, ((2 * w + 1) * DM + 8 * lane) * 2, 0, 0);
    u32x4 wqr[4];
#pragma unroll
    for (int i = 0; i < 4; ++i) wqr[i] = LDB128(rwq, ((128 * sp + 32 * w + 8 * i + (lane >> 3)) * (HQ * DH) + kvh * 512 + 64 * b + 8 * (lane & 7)) * 2, 0, WAUX);
    SB(); RAWBAR(); SB();
    float v0[8], v1[8];
#pragma unroll
    for (int i = 0; i < 4; ++i) { v0[2 * i] = bflo(xr0[i]); v0[2 * i + 1] = bfhi(xr0[i]); v1[2 * i] = bflo(xr1[i]); v1[2 * i + 1] = bfhi(xr1[i]); }
    const u32x4 n0 = ln_row(v0), n1 = ln_row(v1);
    *(LAS u32x4*)(L + L_A + 2 * w * A_STR + lane * 16) = n0;
    *(LAS u32x4*)(L + L_A + (2 * w + 1) * A_STR + lane * 16) = n1;
#pragma unroll
    for (int i = 0; i < 4; ++i) *(LAS u32x4*)(L + L_WQ + (32 * w + 8 * i + (lane >> 3)) * 144 + (lane & 7) * 16) = wqr[i];
    STAMP(12);
    wg_barrier();
    STAMP(13);
    const u32x4 a = *(LAS u32x4*)(L + L_A + (lane & 7) * A_STR + (128 * sp + 32 * w + 8 * g) * 2);
#pragma unroll
    for (int nt = 0; nt < 4; ++nt) {
      LAS unsigned char* p = L + L_WQ + (32 * w + 8 * g + ((lane >> 2) & 3)) * 144 + nt * 32 + (lane & 3) * 8;
      const u32x2 lo = tr_read(p), hi = tr_read(p + 4 * 144);
      const f32x4 acc = mfma32(a, u32x4{lo.x, lo.y, hi.x, hi.y}, f32x4{0.f, 0.f, 0.f, 0.f});
      if (lane < 32) {
#pragma unroll
        for (int r = 0; r < 4; ++r) *(LAS float*)(L + L_P + ((w * 8 + 4 * g + r) * 64 + 16 * nt + c16) * 4) = acc[r];
      }
    }
    wg_barrier();
    STAMP(14);
    asm volatile("s_waitcnt lgkmcnt(0)" : "+s"(E));
    T64 = E + 1;
    const u64 T40 = T64 & 0xFFFFFFFFFFull;
#pragma unroll
    for (int i = 0; i < 2; ++i) {
      const int o = tid + 256 * i;
      float sum = 0.f;
#pragma unroll
      for (int ww = 0; ww < 4; ++ww) sum += *(LAS float*)(L + L_P + (ww * 512 + o) * 4);
      const u32 sv = f2u(sum) + 0x80u;
      __hip_atomic_store((GAS u64*)(wsg + WS_QG) + ((kvh * 8 + (o >> 6)) * 4 + sp) * 512 + 64 * b + (o & 63), (T40 << 24) | (u64)(sv >> 8), RLX);
    }
    STAMP(1);
    SB(); RAWBAR(); SB();
    __builtin_amdgcn_s_sleep(QWAIT);
    {
      GAS u64* qgp = (GAS u64*)(wsg + WS_QG) + (kvh * 8 + b) * 4 * 512 + tid;
      u64 qv[8]; int spins = 0;
      for (;;) {
        bool ok = true;
#pragma unroll
        for (int i = 0; i < 8; ++i) qv[i] = __hip_atomic_load(qgp + (i & 3) * 512 + (i >> 2) * 256, RLX);
#pragma unroll
        for (int i = 0; i < 8; ++i) ok &= (qv[i] >> 24) == T40;
        if (__all(ok) || ++spins > SPIN_MAX) break;
        MEMFENCE(); __builtin_amdgcn_s_sleep(2);
      }
      const float qa = (u2f((u32)qv[0] << 8) + u2f((u32)qv[1] << 8)) + (u2f((u32)qv[2] << 8) + u2f((u32)qv[3] << 8));
      const float qb = (u2f((u32)qv[4] << 8) + u2f((u32)qv[5] << 8)) + (u2f((u32)qv[6] << 8) + u2f((u32)qv[7] << 8));
      *(LAS u16*)(L + L_Q + tid * 2) = (u16)cvtpk(qa, qa);
      *(LAS u16*)(L + L_Q + 512 + tid * 2) = (u16)cvtpk(qb, qb);
    }
    STAMP(15);
    wg_barrier();
    SB();
    if (w == 3) {
      const u32x4 sv = {SENT, SENT, SENT, SENT};
      __builtin_amdgcn_raw_buffer_store_b128(sv, rws, WS_PART + (kvh * 32 + j) * 1024 + lane * 16, 0, AUX_SC1);
      if (role == 1) __builtin_amdgcn_raw_buffer_store_b128(sv, rws, WS_Y + ((kvh * 8 + rb) * DM + 64 * b + 8 * cp) * 2, 0, AUX_SC1);
      if (role == 2) __builtin_amdgcn_raw_buffer_store_b64(u32x2{SENT, SENT}, rws, WS_G + (rb * FF + 32 * cgu + 4 * cp) * 2, 0, AUX_SC1);
    }
    if (role == 1 || !LATEW) { u32x4 wst[16]; LOAD_WEIGHTS(wst); STAGE_WEIGHTS(wst); }
    DRAIN();
    wg_barrier();
  } else {
    const int sw = w - 4;
    const int koff = (4 * (c16 & 3) + (c16 >> 2)) * 256 + g * 16, voff = lane * 16;
    const bool vkeep = b >= VB;
#define ISSUE_K(slot, it) do { const int so_ = ((it) * 4 + sw) * 4096; \
    _Pragma("unroll") for (int u = 0; u < 4; ++u) Kb[slot][u] = LDB128(rk, koff + u * 64, so_, KAUX); SB(); } while (0)
#define ISSUE_V(slot, it) do { const int so_ = ((it) * 4 + sw) * 4096; \
    _Pragma("unroll") for (int u = 0; u < 4; ++u) Vb[slot][u] = LDB128(rv, voff + u * 1024, so_, VAUX); SB(); } while (0)
#define ISSUE(slot, it) do { ISSUE_K(slot, it); ISSUE_V(slot, it); } while (0)
    float mrun = -INFINITY, lrun = 0.f;
    f32x4 O1 = {0.f, 0.f, 0.f, 0.f}, O2 = {0.f, 0.f, 0.f, 0.f};
    auto stream = [&](auto kaux, auto vaux) __attribute__((always_inline)) {
    constexpr int KAUX = decltype(kaux)::value, VAUX = decltype(vaux)::value;
    u32x4 Kb[DEPTH][4], Vb[DEPTH][4];
    RAWBAR();
    if (SLEEP0) __builtin_amdgcn_s_sleep(SLEEP0);
    SB();
    ISSUE(0, 0);
    TSTAMP(12);
    RAWBAR(); SB();
    TSTAMP(13);
    ISSUE_V(1, 1);
    RAWBAR(); SB();
    RAWBAR(); SB();
    ISSUE_K(1, 1);
    asm volatile("s_waitcnt vmcnt(%0)" :: "n"(LAGW) : "memory"); SB();
    TSTAMP(14);
    ISSUE_K(2, 2);
    asm volatile("s_waitcnt vmcnt(%0)" :: "n"(LAGW) : "memory"); SB();
    ISSUE_V(2, 2);
    asm volatile("s_waitcnt vmcnt(%0)" :: "n"(LAGW) : "memory"); SB();
    ISSUE_K(3, 3);
    asm volatile("s_waitcnt vmcnt(%0)" :: "n"(LAGW) : "memory"); SB();
    ISSUE_V(3, 3);
    TSTAMP(15);
    wg_barrier();
    STAMP(2);
    u32x4 Qf[4];
#pragma unroll
    for (int u = 0; u < 4; ++u) Qf[u] = *(LAS u32x4*)(L + L_Q + ((lane & 3) * 128 + 32 * u + 8 * g) * 2);
    const int jx = c16 >> 2;
    const u32 m0k = jx == 0 ? 0x0000FFFFu : jx == 1 ? 0xFFFF0000u : 0u;
    const u32 m1k = jx == 2 ? 0x0000FFFFu : jx == 3 ? 0xFFFF0000u : 0u;
#define PROCESS(slot) do { \
    f32x4 s_ = {0.f, 0.f, 0.f, 0.f}; \
    _Pragma("unroll") for (int u = 0; u < 4; ++u) s_ = mfma32(Kb[slot][u], Qf[u], s_); \
    const float tm_ = xrow_max(fmaxf(fmaxf(s_[0], s_[1]), fmaxf(s_[2], s_[3]))); \
    if (__any(tm_ > mrun)) { \
      const float mn_ = fmaxf(mrun, tm_), al_ = __builtin_amdgcn_exp2f((mrun - mn_) * SC_LOG2); \
      lrun *= al_; mrun = mn_; \
      const float a0_ = dpp<0x00>(al_), a1_ = dpp<0x55>(al_), a2_ = dpp<0xAA>(al_), a3_ = dpp<0xFF>(al_); \
      O1[0] *= a0_; O1[1] *= a1_; O1[2] *= a2_; O1[3] *= a3_; O2[0] *= a0_; O2[1] *= a1_; O2[2] *= a2_; O2[3] *= a3_; \
    } \
    const float mc_ = mrun * SC_LOG2; \
    _Pragma("unroll") for (int i = 0; i < 4; ++i) { \
      const float p_ = __builtin_amdgcn_exp2f(fmaf(s_[i], SC_LOG2, -mc_)); lrun += p_; \
      const u32 pv_ = cvtpk(p_, p_); const u32x2 a_ = {pv_ & m0k, pv_ & m1k}; \
      O1 = mfma16(a_, u32x2{Vb[slot][i].x, Vb[slot][i].y}, O1); O2 = mfma16(a_, u32x2{Vb[slot][i].z, Vb[slot][i].w}, O2); \
    } SB(); } while (0)
#pragma unroll 1
    for (int it = 0; it < 28; it += 4) { PROCESS(0); ISSUE(0, it + 4); PROCESS(1); ISSUE(1, it + 5); PROCESS(2); ISSUE(2, it + 6); PROCESS(3); ISSUE(3, it + 7); }
    PROCESS(0); ISSUE(0, 32); PROCESS(1);
    if (30 * 4 + sw < ntile) PROCESS(2);
    if (31 * 4 + sw < ntile) PROCESS(3);
    if (32 * 4 + sw < ntile) PROCESS(0);
    };
    if (vkeep) stream(ic<0>{}, ic<0>{}); else stream(ic<AUX_NT>{}, ic<AUX_NT>{});
    asm volatile("s_waitcnt lgkmcnt(0)" : "+s"(E));
    T64 = E + 1;
    STAMP(3);
    lrun = xrow_sum(lrun);
#pragma unroll
    for (int h = 0; h < 4; ++h) *(LAS f32x2*)(L + L_MRG + ((sw * 4 + h) * 64 + lane) * 8) = f32x2{O1[h], O2[h]};
    if (lane < 4) { *(LAS float*)(L + L_ST + (lane * 4 + sw) * 4) = mrun; *(LAS float*)(L + L_ST + 64 + (lane * 4 + sw) * 4) = lrun; }
    wg_barrier();
  }
  const u32 T32 = (u32)T64;

  if (w == 1) { if (E1SLEEP) __builtin_amdgcn_s_sleep(E1SLEEP); if (lane == 0) __hip_atomic_store((GAS u64*)(wsg + WS_H1E + (kvh * 32 + j) * FS), T64, RLX); }
  if (w == 0) {
    const int h = g;
    const f32x4 ma = *(LAS f32x4*)(L + L_ST + h * 16), la = *(LAS f32x4*)(L + L_ST + 64 + h * 16);
    const float M = fmaxf(fmaxf(ma[0], ma[1]), fmaxf(ma[2], ma[3]));
    float Ls = 0.f;
    f32x4 oa = {0.f, 0.f, 0.f, 0.f}, ob = {0.f, 0.f, 0.f, 0.f};
#pragma unroll
    for (int ww = 0; ww < 4; ++ww) {
      const float f = __builtin_amdgcn_exp2f((ma[ww] - M) * SC_LOG2);
      Ls = fmaf(f, la[ww], Ls);
      oa += f * *(LAS f32x4*)(L + L_MRG + ((ww * 4 + h) * 64 + 4 * c16) * 8);
      ob += f * *(LAS f32x4*)(L + L_MRG + ((ww * 4 + h) * 64 + 4 * c16) * 8 + 16);
    }
    const float inv = __builtin_amdgcn_rcpf(Ls);
    const u32x4 pk = {nosent(cvtpk(oa[0] * inv, oa[1] * inv)), nosent(cvtpk(oa[2] * inv, oa[3] * inv)), nosent(cvtpk(ob[0] * inv, ob[1] * inv)), nosent(cvtpk(ob[2] * inv, ob[3] * inv))};
    __builtin_amdgcn_raw_buffer_store_b128(pk, rws, WS_PART + (kvh * 32 + j) * 1024 + lane * 16, 0, AUX_SC1);
    const float lam = fmaf(M, SC_LOG2, __builtin_amdgcn_logf(Ls));
    EARLY_DRAIN();
    if (c16 == 0) __hip_atomic_store((GAS u64*)(wsg + WS_H1F + (kvh * 32 + j) * FS) + h, ((u64)T32 << 32) | f2u(lam), RLX);
  }
  STAMP(4);
  if (LATEW && role >= 2) {
    if (tid == 0) *(LAS u32*)(L + L_CNT + 8) = 0u;
    SB(); RAWBAR(); SB();
    if (w < 4) { u32x4 wst[16]; LOAD_WEIGHTS(wst); STAGE_WEIGHTS(wst); }
    wg_barrier();
  }

  if (role == 1) {
    u32x4 Bf[16];
    if (w < 4) {
      const int q = (lane >> 2) & 3;
#pragma unroll
      for (int k = 0; k < 16; ++k) {
        const int d0 = (k >> 2) * 128 + 32 * g + 8 * (q >> 1) + 4 * (q & 1) + (k & 3), d1 = d0 + 16;
        const int sg = (q >> 1) | ((g & 1) << 1);
        const u32x2 lo = tr_read(L + L_ROLE + (d0 ^ (q & 1)) * 128 + 32 * (w ^ sg) + (lane & 3) * 8);
        const u32x2 hi = tr_read(L + L_ROLE + (d1 ^ (q & 1)) * 128 + 32 * (w ^ sg) + (lane & 3) * 8);
        Bf[k] = u32x4{lo.x, lo.y, hi.x, hi.y};
      }
    } else {
#pragma unroll
      for (int k = 0; k < 16; ++k) Bf[k] = u32x4{0u, 0u, 0u, 0u};
    }
    GAS u64* fp = (GAS u64*)(wsg + WS_H1F + (kvh * 32 + 4 * w) * FS) + g;
    const int po = WS_PART + (kvh * 32 + 4 * w) * 1024 + lane * 16;
    LAS unsigned char* pl = L + L_P + w * P_STR + lane * 16;
    LAS unsigned char* al = L + L_P + (lane & 7) * P_STR + g * 16;
    int spins = 0;
    {
      GAS u64* ep = (GAS u64*)(wsg + WS_H1E + (kvh * 32 + 4 * w + (lane & 3)) * FS);
      for (;;) {
        if (__all(__hip_atomic_load(ep, RLX) == T64) || ++spins > SPIN_MAX) break;
        MEMFENCE(); __builtin_amdgcn_s_sleep(1);
      }
    }
    MEMFENCE();
    TSTAMP(12);
    spins = 0;
    for (;;) {
      const u64 f0 = __hip_atomic_load(fp, RLX), f1 = __hip_atomic_load(fp + FS / 8, RLX), f2 = __hip_atomic_load(fp + 2 * FS / 8, RLX), f3 = __hip_atomic_load(fp + 3 * FS / 8, RLX);
      u32x4 pv[4];
#pragma unroll
      for (int i = 0; i < 4; ++i) pv[i] = LDB128(rws, po + i * 1024, 0, AUX_SC1);
      SB();
      const float l0 = u2f((u32)f0), l1 = u2f((u32)f1), l2 = u2f((u32)f2), l3 = u2f((u32)f3);
      const float lm = fmaxf(fmaxf(l0, l1), fmaxf(l2, l3));
      float e[4] = {__builtin_amdgcn_exp2f(l0 - lm), __builtin_amdgcn_exp2f(l1 - lm), __builtin_amdgcn_exp2f(l2 - lm), __builtin_amdgcn_exp2f(l3 - lm)};
      const float inv = __builtin_amdgcn_rcpf((e[0] + e[1]) + (e[2] + e[3]));
#pragma unroll
      for (int i = 0; i < 4; ++i) e[i] *= inv;
      SB();
      u32 m = 0; f32x2 o[4];
#pragma unroll
      for (int d = 0; d < 4; ++d) o[d] = f32x2{0.f, 0.f};
#pragma unroll
      for (int i = 0; i < 4; ++i) {
        m = mx4(m, pv[i]);
#pragma unroll
        for (int d = 0; d < 4; ++d) o[d] += e[i] * f32x2{bflo(pv[i][d]), bfhi(pv[i][d])};
      }
      *(LAS u32x4*)pl = u32x4{cvtpk(o[0][0], o[0][1]), cvtpk(o[1][0], o[1][1]), cvtpk(o[2][0], o[2][1]), cvtpk(o[3][0], o[3][1])};
      const bool ok = (m != SENT) & ((u32)(f0 >> 32) == T32) & ((u32)(f1 >> 32) == T32) & ((u32)(f2 >> 32) == T32) & ((u32)(f3 >> 32) == T32);
      if (__all(ok) || ++spins > SPIN_MAX) break;
      MEMFENCE();
    }
    RAWSTAMP(10, spins + 1);
    TSTAMP(13);
    wg_barrier();
    if (w == 1 && E2EARLY) { if (E2SLEEP) __builtin_amdgcn_s_sleep(E2SLEEP); if (lane < 8) __hip_atomic_store((GAS u64*)(wsg + WS_H2F + (lane * 64 + kvh * 8 + b) * FS), T64, RLX); }
    TSTAMP(14);
    if (w < 4) {
      u32x4 av[16];
#pragma unroll
      for (int k = 0; k < 16; ++k) av[k] = *(LAS u32x4*)(al + k * 64);
      SB();
      f32x4 a0 = {0.f, 0.f, 0.f, 0.f}, a1 = {0.f, 0.f, 0.f, 0.f};
#pragma unroll
      for (int k = 0; k < 16; k += 2) { a0 = mfma32(av[k], Bf[k], a0); a1 = mfma32(av[k + 1], Bf[k + 1], a1); }
      const float yv4[4] = {a0[0] + a1[0], a0[1] + a1[1], a0[2] + a1[2], a0[3] + a1[3]};
      const bool odd = lane & 1;
      const u32x2 pk = pack_pairs(yv4, odd);
      const int yo = WS_Y + ((kvh * 8 + 4 * g + (odd ? 2 : 0)) * DM + 64 * b + 16 * w + (c16 & 14)) * 2;
      if (lane < 32) { __builtin_amdgcn_raw_buffer_store_b32(pk.x, rws, yo, 0, AUX_SC1); __builtin_amdgcn_raw_buffer_store_b32(pk.y, rws, yo + DM * 2, 0, AUX_SC1); }
    }
    if (!E2EARLY) { DRAIN(); wg_barrier(); if (w == 0 && lane < 8) __hip_atomic_store((GAS u64*)(wsg + WS_H2F + (lane * 64 + kvh * 8 + b) * FS), T64, RLX); }
    STAMP(5);
    STAMP(6);
  } else if (role == 2) {
    u32x4 Bf[16];
    if (w < 4) {
      const int q = (lane >> 2) & 3, p = lane & 3;
#pragma unroll
      for (int k = 0; k < 16; ++k) {
        LAS unsigned char* pp = L + L_ROLE + (p >> 1) * 32768 + (32 * k + 8 * g + q) * 64 + 32 * ((w >> 1) ^ (g & 1)) + 16 * (w & 1) + 8 * (p & 1);
        const u32x2 lo = tr_read(pp), hi = tr_read(pp + 256);
        Bf[k] = u32x4{lo.x, lo.y, hi.x, hi.y};
      }
    } else {
#pragma unroll
      for (int k = 0; k < 16; ++k) Bf[k] = u32x4{0u, 0u, 0u, 0u};
    }
    const u32x4 xv = LDB128(rx, (w * DM + 8 * lane) * 2, 0, 0);
    float x8[8];
#pragma unroll
    for (int i = 0; i < 4; ++i) { x8[2 * i] = bflo(xv[i]); x8[2 * i + 1] = bfhi(xv[i]); }
    const int yo = WS_Y + (w * DM + 8 * lane) * 2;
    LAS unsigned char* aw = L + L_A + w * A_STR + lane * 16;
    LAS unsigned char* ar = L + L_A + (lane & 7) * A_STR + 8 * g * 2;
    u32x4 yv[8];
    {
      GAS u64* fp = (GAS u64*)(wsg + WS_H2F + (kvh * 64 + lane) * FS);
      LAS u32* mk = (LAS u32*)(L + L_CNT + 8);
      u32 done = 0; int spins = 0;
#pragma unroll
      for (int i = 0; i < 8; ++i) yv[i] = u32x4{0u, 0u, 0u, 0u};
      for (;;) {
        u32 rdy;
        if (w == 0) {
          const u64 bal = __builtin_amdgcn_ballot_w64(__hip_atomic_load(fp, RLX) == T64);
          rdy = 0;
#pragma unroll
          for (int i = 0; i < 8; ++i) rdy |= (((bal >> (8 * i)) & 0xFFull) == 0xFFull ? 1u : 0u) << i;
          if (rdy != done && lane == 0) __hip_atomic_store(mk, rdy, __ATOMIC_RELAXED, __HIP_MEMORY_SCOPE_WORKGROUP);
        } else {
          rdy = __builtin_amdgcn_readfirstlane(__hip_atomic_load(mk, __ATOMIC_RELAXED, __HIP_MEMORY_SCOPE_WORKGROUP));
        }
#pragma unroll
        for (int i = 0; i < 8; ++i)
          if (((rdy & ~done) >> i) & 1u) yv[i] = LDB128(rws, yo + i * (8 * DM * 2), 0, AUX_SC1);
        done |= rdy;
        if (done == 0xFFu || ++spins > SPIN_MAX * 8) break;
        MEMFENCE(); if (w == 0) { if (PSLEEP) __builtin_amdgcn_s_sleep(PSLEEP); } else __builtin_amdgcn_s_sleep(LSLEEP);
      }
    }
    STAMP(7);
    {
      int spins = 0;
      for (;;) {
        u32 bad = 0;
#pragma unroll
        for (int i = 0; i < 8; ++i) if (!__all(mx4(0u, yv[i]) != SENT)) bad |= 1u << i;
        if (bad == 0 || ++spins > SPIN_MAX) break;
        MEMFENCE();
#pragma unroll
        for (int i = 0; i < 8; ++i) if ((bad >> i) & 1u) yv[i] = LDB128(rws, yo + i * (8 * DM * 2), 0, AUX_SC1);
        RAWSTAMP(11, spins + 1);
      }
    }
    float v[8];
#pragma unroll
    for (int e = 0; e < 8; ++e) v[e] = x8[e];
#pragma unroll
    for (int i = 0; i < 8; ++i) {
#pragma unroll
      for (int e = 0; e < 4; ++e) { v[2 * e] += bflo(yv[i][e]); v[2 * e + 1] += bfhi(yv[i][e]); }
    }
    TSTAMP(13);
    *(LAS u32x4*)aw = ln_row(v);
    TSTAMP(14);
    wg_barrier();
    if (w == 1 && E3EARLY) { if (E3SLEEP) __builtin_amdgcn_s_sleep(E3SLEEP); if (lane < 8) __hip_atomic_store((GAS u64*)(wsg + WS_H3F + (lane * 64 + cgu) * FS), T64, RLX); }
    STAMP(8);
    if (w < 4) {
      u32x4 av[16];
#pragma unroll
      for (int k = 0; k < 16; ++k) av[k] = *(LAS u32x4*)(ar + k * 64);
      SB();
      f32x4 a0 = {0.f, 0.f, 0.f, 0.f}, a1 = {0.f, 0.f, 0.f, 0.f};
#pragma unroll
      for (int k = 0; k < 16; k += 2) { a0 = mfma32(av[k], Bf[k], a0); a1 = mfma32(av[k + 1], Bf[k + 1], a1); }
      float r4[4];
#pragma unroll
      for (int e = 0; e < 4; ++e) {
        const float z = a0[e] + a1[e];
        const float up = dpp<0x128>(z);
        const float t = z * fmaf(z * z, 0.044715f * 0.7978845608028654f, 0.7978845608028654f);
        const float sg = __builtin_amdgcn_rcpf(1.0f + __builtin_amdgcn_exp2f(t * (-2.0f * 1.4426950408889634f)));
        r4[e] = z * sg * up;
      }
      const bool odd = lane & 1;
      const u32x2 pk = pack_pairs(r4, odd);
      const int gof = WS_G + ((4 * g + (odd ? 2 : 0)) * FF + 32 * cgu + 8 * w + (c16 & 6)) * 2;
      if (lane < 32 && c16 < 8) { __builtin_amdgcn_raw_buffer_store_b32(pk.x, rws, gof, 0, AUX_SC1); __builtin_amdgcn_raw_buffer_store_b32(pk.y, rws, gof + FF * 2, 0, AUX_SC1); }
    }
    if (!E3EARLY) { DRAIN(); wg_barrier(); if (w == 0 && lane < 8) __hip_atomic_store((GAS u64*)(wsg + WS_H3F + (lane * 64 + cgu) * FS), T64, RLX); }
    STAMP(9);
  } else if (role == 3) {
    u32x4 Bf[7];
#pragma unroll
    for (int u = 0; u < 7; ++u) Bf[u] = bfrag(L + L_ROLE + (224 * w + 32 * u) * 32 + trl, 128);
    int go[4]; LAS unsigned char* gl[4];
#pragma unroll
    for (int i = 0; i < 4; ++i) {
      const int ci = (w + 8 * i) * 64 + lane, row = ci / 224; const bool in = i < 3 || w < 4;
      go[i] = in ? WS_G + ci * 16 : WS_END;
      gl[i] = L + L_P + (in ? row * G_STR + (ci - row * 224) * 16 : 8 * G_STR + lane * 16);
    }
    LAS unsigned char* ar = L + L_P + (lane & 7) * G_STR + (7 * w * 32 + 8 * g) * 2;
    LAS unsigned char* rw = L + L_RED + ((w * 8 + 4 * g) * 16 + c16) * 4;
    f32x2 hres = {0.f, 0.f};
    if (w == 0) {
      const u32 xw = *(const GAS u32*)((const GAS unsigned char*)x + (rb * DM + 16 * cdn + 2 * cp) * 2);
      GAS u64* fp = (GAS u64*)(wsg + WS_H2F + (kvh * 64 + (lane & 7) * 8 + (cdn >> 2)) * FS); int spins = 0;
      for (;;) { const u64 f0 = __hip_atomic_load(fp, RLX); if (__all(f0 == T64) || ++spins > SPIN_MAX) break; MEMFENCE(); __builtin_amdgcn_s_sleep(2); }
      MEMFENCE();
      for (spins = 0;; ) {
        u32 yy[8], m = 0;
#pragma unroll
        for (int q = 0; q < 8; ++q) yy[q] = __builtin_amdgcn_raw_buffer_load_b32(rws, WS_Y + ((q * 8 + rb) * DM + 16 * cdn + 2 * cp) * 2, 0, AUX_SC1);
        hres = f32x2{bflo(xw), bfhi(xw)};
#pragma unroll
        for (int q = 0; q < 8; ++q) { m = umax(m, yy[q]); hres += f32x2{bflo(yy[q]), bfhi(yy[q])}; }
        if (__all(m != SENT) || ++spins > SPIN_MAX) break;
        MEMFENCE();
      }
      GAS u64* gp = (GAS u64*)(wsg + WS_H3F + (kvh * 64 + lane) * FS); spins = 0;
      for (;;) {
        const u64 f0 = lane < NGU ? __hip_atomic_load(gp, RLX) : T64;
        if (__all(f0 == T64) || ++spins > SPIN_MAX) break;
        MEMFENCE(); if (PSLEEP) __builtin_amdgcn_s_sleep(PSLEEP);
      }
      if (cdn == 0 && lane == 0) __hip_atomic_store((GAS u64*)(wsg + WS_E), T64, RLX);
    }
    wg_barrier();
    STAMP(9);
    for (int spins = 0;; ) {
      u32x4 gv[4];
#pragma unroll
      for (int i = 0; i < 4; ++i) gv[i] = LDB128(rws, go[i], 0, AUX_SC1);
      SB();
      u32 m = 0;
#pragma unroll
      for (int i = 0; i < 4; ++i) { m = mx4(m, gv[i]); *(LAS u32x4*)gl[i] = gv[i]; }
      if (__all(m != SENT) || ++spins > SPIN_MAX) break;
      RAWSTAMP(11, spins + 1);
      MEMFENCE();
    }
    TSTAMP(12);
    wg_barrier();
    TSTAMP(13);
    {
      u32x4 av[7];
#pragma unroll
      for (int u = 0; u < 7; ++u) av[u] = *(LAS u32x4*)(ar + u * 64);
      SB();
      f32x4 a0 = {0.f, 0.f, 0.f, 0.f}, a1 = {0.f, 0.f, 0.f, 0.f};
#pragma unroll
      for (int u = 0; u < 7; ++u) { if (u & 1) a1 = mfma32(av[u], Bf[u], a1); else a0 = mfma32(av[u], Bf[u], a0); }
      if (lane < 32) {
#pragma unroll
        for (int r = 0; r < 4; ++r) *(LAS float*)(rw + r * 64) = a0[r] + a1[r];
      }
    }
    TSTAMP(14);
    wg_barrier();
    TSTAMP(15);
    if (w == 0) {
      f32x2 rr[8];
#pragma unroll
      for (int ww = 0; ww < 8; ++ww) rr[ww] = *(LAS f32x2*)(L + L_RED + ((ww * 8 + rb) * 16 + 2 * cp) * 4);
      const f32x2 sm = hres + (((rr[0] + rr[1]) + (rr[2] + rr[3])) + ((rr[4] + rr[5]) + (rr[6] + rr[7])));
      *(GAS u32*)((GAS unsigned char*)out + (rb * DM + 16 * cdn + 2 * cp) * 2) = cvtpk(sm[0], sm[1]);
    }
    STAMP(8);
  }
  STAMP_FLUSH();
}

extern "C" void kernel_launch(void* const* d_in, const int* in_sizes, int n_in, void* d_out, int out_size, void* d_ws, size_t ws_size, hipStream_t stream) {
  fused_decode_block<<<NWG, NTHR, 0, stream>>>((const u16*)d_in[0], (const u16*)d_in[1], (const u16*)d_in[2], (const u16*)d_in[3], (const u16*)d_in[4],
                                              (const u16*)d_in[6], (const u16*)d_in[7], (const u16*)d_in[8], (u16*)d_out, (unsigned char*)d_ws);
}
```

```cpp
#include <hip/hip_runtime.h>
#include <hip/hip_bf16.h>
#include <stdint.h>

typedef unsigned short u16;
typedef unsigned u32;
typedef unsigned long long u64;
typedef u32 u32x4 __attribute__((ext_vector_type(4)));
typedef u32 u32x2 __attribute__((ext_vector_type(2)));
typedef float f32x4 __attribute__((ext_vector_type(4)));
typedef float f32x2 __attribute__((ext_vector_type(2)));
typedef short s16x8 __attribute__((ext_vector_type(8)));
typedef short s16x4 __attribute__((ext_vector_type(4)));
typedef __bf16 bf16x8_t __attribute__((ext_vector_type(8)));
#define GAS __attribute__((address_space(1)))
#define LAS __attribute__((address_space(3)))
#define RLX __ATOMIC_RELAXED, __HIP_MEMORY_SCOPE_AGENT

#define QPOS 0
#define HOLD 1
#define PASSES 1
#define SLEEP0 8
#define QSLEEP 4
#define LATEW 1
#define VB 8
#define KB 8
#define WAUX 2
#define FDELAY 0
#define PSLEEP 1
#define NOQ 0
#define THROTTLE 1
#define B3 1
#define QWAIT 30
#define DEPTH 4
#define LAGW 24
#define WOEARLY 0
#define E1SLEEP 4
#define E2EARLY 1
#define E2SLEEP 3
#define E3EARLY 1
#define E3SLEEP 3
#define LSLEEP 1
#define TGT 3800
#define EARLY 1
#define EARLY_DRAIN() do { if (FDELAY) __builtin_amdgcn_s_sleep(FDELAY); } while (0)
#define STAMPS 0

constexpr int NB = 8, DM = 512, HQ = 32, HKV = 8, SK = 8192, DH = 128, FF = 1792;
constexpr int NWG = 256, NTHR = 512;
constexpr int NGU = FF / 32;
constexpr int TOK_WG = SK / 4;
#define GUD 4
static_assert(GUD % 4 == 0 && GUD <= 8, "every wave has 30..33 tiles");
constexpr float SC_LOG2 = 0.08838834764831845f * 1.4426950408889634f;
constexpr float LN_EPS = 1e-5f;
constexpr int AUX_NT = 2, AUX_SC1 = 16;
constexpr int SPIN_MAX = 1 << 16;

constexpr int FS = 256;
constexpr int WS_E    = 0;
constexpr int WS_H1F  = 4096;
constexpr int WS_H1E  = WS_H1F + 256 * FS;
constexpr int WS_H2F  = WS_H1E + 256 * FS;
constexpr int WS_H3F  = WS_H2F + 512 * FS;
constexpr int WS_QG   = WS_H3F + 512 * FS;
constexpr int WS_PART = WS_QG + HKV * NB * 4 * 512 * 8;
constexpr int WS_Y    = WS_PART + HKV * 32 * 4 * 128 * 2;
constexpr int WS_G    = WS_Y + HKV * NB * DM * 2;
constexpr int WS_END  = WS_G + NB * FF * 2;
constexpr int WS_STAMP = 2 << 20;

constexpr int L_ROLE = 0;
constexpr int L_WQ = 0;
constexpr int L_MRG = 65536;
constexpr int A_STR = 1040;
constexpr int L_A = L_MRG + 16384;
constexpr int L_RED = L_A + 8 * A_STR;
constexpr int L_Q = L_RED + 8192;
constexpr int L_ST = L_Q + 1024;
constexpr int L_CNT = L_ST + 192;
constexpr int L_LAM = L_ST + 256;
constexpr int P_STR = 1040;
constexpr int L_P = L_LAM + 512;
constexpr int G_STR = 3600;
constexpr int LDS_BYTES = L_P + 32 * P_STR;

typedef __bf16 bf16x2_t __attribute__((ext_vector_type(2)));
__device__ __forceinline__ u32 cvtpk(float lo, float hi) { const bf16x2_t r = {(__bf16)lo, (__bf16)hi}; return __builtin_bit_cast(u32, r); }
__device__ __forceinline__ float u2f(u32 v) { return __builtin_bit_cast(float, v); }
__device__ __forceinline__ u32 f2u(float v) { return __builtin_bit_cast(u32, v); }
__device__ __forceinline__ float bflo(u32 v) { return __builtin_bit_cast(float, v << 16); }
__device__ __forceinline__ float bfhi(u32 v) { return __builtin_bit_cast(float, v & 0xffff0000u); }
template <int CTRL> __device__ __forceinline__ float dpp(float x) { return __builtin_bit_cast(float, __builtin_amdgcn_mov_dpp(__builtin_bit_cast(int, x), CTRL, 0xf, 0xf, true)); }
__device__ __forceinline__ float xrow_max(float x) {
  auto s = __builtin_amdgcn_permlane16_swap(f2u(x), f2u(x), false, false);
  x = fmaxf(u2f(s[0]), u2f(s[1]));
  auto t = __builtin_amdgcn_permlane32_swap(f2u(x), f2u(x), false, false);
  return fmaxf(u2f(t[0]), u2f(t[1]));
}
__device__ __forceinline__ float xrow_sum(float x) {
  auto s = __builtin_amdgcn_permlane16_swap(f2u(x), f2u(x), false, false);
  x = u2f(s[0]) + u2f(s[1]);
  auto t = __builtin_amdgcn_permlane32_swap(f2u(x), f2u(x), false, false);
  return u2f(t[0]) + u2f(t[1]);
}
__device__ __forceinline__ float wave_sum(float x) {
  x += dpp<0xB1>(x); x += dpp<0x4E>(x); x += dpp<0x141>(x); x += dpp<0x140>(x);
  return xrow_sum(x);
}
__device__ __forceinline__ f32x4 mfma32(u32x4 a, u32x4 b, f32x4 c) {
  return __builtin_amdgcn_mfma_f32_16x16x32_bf16(__builtin_bit_cast(bf16x8_t, a), __builtin_bit_cast(bf16x8_t, b), c, 0, 0, 0);
}
__device__ __forceinline__ f32x4 mfma16(u32x2 a, u32x2 b, f32x4 c) {
  return __builtin_amdgcn_mfma_f32_16x16x16bf16_1k(__builtin_bit_cast(s16x4, a), __builtin_bit_cast(s16x4, b), c, 0, 0, 0);
}
typedef short v4i16_t __attribute__((ext_vector_type(4)));
__device__ __forceinline__ u32x2 tr_read(LAS unsigned char* p) {
  return __builtin_bit_cast(u32x2, __builtin_amdgcn_ds_read_tr16_b64_v4i16((LAS v4i16_t*)p));
}
__device__ __forceinline__ u32x4 bfrag(LAS unsigned char* p, int second_off) {
  const u32x2 a = tr_read(p), b = tr_read(p + second_off);
  return u32x4{a.x, a.y, b.x, b.y};
}
constexpr u32 SENT = 0xFFFFFFFFu;
__device__ __forceinline__ u32 nosent(u32 d) { return d == SENT ? 0x7FFF7FFFu : d; }
__device__ __forceinline__ u32 umax(u32 a, u32 b) { return a > b ? a : b; }
__device__ __forceinline__ u32 mx4(u32 m, u32x4 v) { return umax(umax(umax(m, v.x), v.y), umax(v.z, v.w)); }
__device__ __forceinline__ u32x2 pack_pairs(const float (&a)[4], bool odd) {
  const float n0 = dpp<0xB1>(odd ? a[0] : a[2]), n1 = dpp<0xB1>(odd ? a[1] : a[3]);
  const float o0 = odd ? a[2] : a[0], o1 = odd ? a[3] : a[1];
  return u32x2{nosent(cvtpk(odd ? n0 : o0, odd ? o0 : n0)), nosent(cvtpk(odd ? n1 : o1, odd ? o1 : n1))};
}
__device__ __forceinline__ __amdgpu_buffer_rsrc_t mkrs(const void* p, u32 bytes) { return __builtin_amdgcn_make_buffer_rsrc((void*)p, 0, bytes, 0x00020000); }
template <int V> struct ic { static constexpr int value = V; };
#define LDB128(rs, vo, so, aux) __builtin_amdgcn_raw_buffer_load_b128(rs, vo, so, aux)
#define SB() __builtin_amdgcn_sched_barrier(0)
#define DRAIN() asm volatile("s_waitcnt vmcnt(0)" ::: "memory")
#define MEMFENCE() asm volatile("" ::: "memory")
#define RAWBAR() asm volatile("s_barrier" ::: "memory")
__device__ __forceinline__ void wg_barrier() { asm volatile("s_waitcnt lgkmcnt(0)\n\ts_barrier" ::: "memory"); }

__device__ __forceinline__ u32x4 ln_row(const float (&v)[8]) {
  const float piv = u2f(__builtin_amdgcn_readfirstlane(f2u(v[0])));
  float d[8], s = 0.f, q = 0.f;
#pragma unroll
  for (int i = 0; i < 8; ++i) { d[i] = v[i] - piv; s += d[i]; q = fmaf(d[i], d[i], q); }
  s += dpp<0xB1>(s); q += dpp<0xB1>(q); s += dpp<0x4E>(s); q += dpp<0x4E>(q); s += dpp<0x141>(s); q += dpp<0x141>(q); s += dpp<0x140>(s); q += dpp<0x140>(q);
  s = xrow_sum(s); q = xrow_sum(q);
  const float mean = s * (1.0f / DM);
  const float var = fmaxf(fmaf(-mean, mean, q * (1.0f / DM)), 0.f);
  const float rs = __builtin_amdgcn_rsqf(var + LN_EPS), off = -mean * rs;
  return u32x4{cvtpk(fmaf(d[0], rs, off), fmaf(d[1], rs, off)), cvtpk(fmaf(d[2], rs, off), fmaf(d[3], rs, off)), cvtpk(fmaf(d[4], rs, off), fmaf(d[5], rs, off)), cvtpk(fmaf(d[6], rs, off), fmaf(d[7], rs, off))};
}

#define STAMP(i) do {} while (0)
#define RAWSTAMP(i, v) do {} while (0)
#define TSTAMP(i) do {} while (0)
#define STAMP_FLUSH() do {} while (0)

__global__ void __launch_bounds__(NTHR, 2)
fused_decode_block(const u16* x_, const u16* Kc_, const u16* Vc_, const u16* wq_, const u16* wo_,
                   const u16* wg_, const u16* wu_, const u16* wd_, u16* out_, unsigned char* ws_) {
  const u16 *x = x_, *Kc = Kc_, *Vc = Vc_, *wq = wq_, *wo = wo_, *wg = wg_, *wu = wu_, *wd = wd_; u16* out = out_; unsigned char* ws = ws_;
  asm volatile("" : "+s"(x), "+s"(Kc), "+s"(Vc), "+s"(wq), "+s"(wo), "+s"(wg), "+s"(wu), "+s"(wd), "+s"(out), "+s"(ws));
  __shared__ __attribute__((aligned(16))) unsigned char lds_raw[LDS_BYTES];
  LAS unsigned char* L = (LAS unsigned char*)lds_raw;
  const int tid = threadIdx.x, lane = tid & 63, w = __builtin_amdgcn_readfirstlane(tid >> 6);
  const int blk = blockIdx.x, kvh = blk & 7, j = blk >> 3, b = j >> 2, sp = j & 3;
  const int g = lane >> 4, c16 = lane & 15;
  GAS unsigned char* wsg = (GAS unsigned char*)ws;
  STAMP(0);

  u64 E;
  asm volatile("s_load_dwordx2 %0, %1, 0x0" : "=s"(E) : "s"(ws));
  const __amdgpu_buffer_rsrc_t rx = mkrs(x, NB * DM * 2);
  const __amdgpu_buffer_rsrc_t rwq = mkrs(wq, DM * HQ * DH * 2);
  const int xt = b < 7 ? 130 + GUD / 4 : 130;
  const int nt0 = xt - 8, nt1 = b < 7 ? xt - GUD : xt;
  const int nextra = (sp == 0 ? nt0 : (sp == 1 ? nt1 : xt)) - 120;
  const int extra0 = 480 + (sp == 0 ? 0 : (sp == 1 ? nt0 - 120 : nt0 + nt1 - 240 + (sp - 2) * (xt - 120)));
  const size_t kvbase = (size_t)(b * HKV + kvh) * SK * DH;
  const __amdgpu_buffer_rsrc_t rk = mkrs(Kc + kvbase, SK * DH * 2);
  const __amdgpu_buffer_rsrc_t rv = mkrs(Vc + kvbase, SK * DH * 2);
  const __amdgpu_buffer_rsrc_t rws = mkrs(ws, WS_END);

  const int trl = (8 * g + ((lane >> 2) & 3)) * 32 + (lane & 3) * 8;
  const int role = sp == 0 ? 1 : sp == 1 ? (b < 7 ? 2 : 3) : (sp == 2 && b < 3 ? 3 : 0);
  const int cgu = kvh * 7 + b, cdn = kvh * 4 + (sp == 1 ? 0 : 1 + b);
#define LOAD_WEIGHTS(wst) do { \
      const u16* p0 = role == 1 ? wo : (role == 2 ? wg : wd); \
      const u16* p1 = role == 2 ? wu : p0; \
      const u32 n0b = role == 0 ? 0u : (role == 1 ? (u32)(HQ * DH * DM * 2) : (u32)(DM * FF * 2)); \
      const u32 n1b = role == 3 ? 0u : n0b; \
      const __amdgpu_buffer_rsrc_t r0 = mkrs(p0, n0b), r1 = mkrs(p1, n0b), r2 = mkrs(p1, n1b); \
      const int v0o = role == 1 ? ((kvh * 512 + 128 * w + (lane >> 3)) * DM + 64 * b + 8 * (lane & 7)) * 2 \
                    : role == 2 ? ((128 * w + (lane >> 2)) * FF + 32 * cgu + 8 * (lane & 3)) * 2 \
                                : ((448 * w + (lane >> 1)) * DM + 16 * cdn + 8 * (lane & 1)) * 2; \
      const int st = role == 1 ? 8 * DM * 2 : role == 2 ? 16 * FF * 2 : 32 * DM * 2; \
      const int v8o = role == 2 ? v0o : v0o + 8 * st; \
      _Pragma("unroll") for (int i = 0; i < 8; ++i) wst[i] = LDB128(r0, v0o + i * st, 0, WAUX); \
      _Pragma("unroll") for (int i = 0; i < 6; ++i) wst[8 + i] = LDB128(r1, v8o + i * st, 0, WAUX); \
      _Pragma("unroll") for (int i = 6; i < 8; ++i) wst[8 + i] = LDB128(r2, v8o + i * st, 0, WAUX); \
      SB(); } while (0)
#define STAGE_WEIGHTS(wst) do { \
    if (role == 1) { \
      _Pragma("unroll") for (int i = 0; i < 16; ++i) { const int r = 128 * w + 8 * i + (lane >> 3), ch = lane & 7; \
        *(LAS u32x4*)(L + L_ROLE + (r ^ ((r >> 2) & 1)) * 128 + 32 * ((ch >> 1) ^ (((r >> 3) & 1) | (((r >> 5) & 1) << 1))) + 16 * (ch & 1)) = wst[i]; } \
    } else if (role == 2) { \
      _Pragma("unroll") for (int i = 0; i < 16; ++i) { const int r = 128 * w + 16 * (i & 7) + (lane >> 2), ch = lane & 3; \
        *(LAS u32x4*)(L + L_ROLE + (i >> 3) * 32768 + r * 64 + 32 * ((ch >> 1) ^ ((r >> 3) & 1)) + 16 * (ch & 1)) = wst[i]; } \
    } else if (role == 3) { \
      _Pragma("unroll") for (int i = 0; i < 14; ++i) *(LAS u32x4*)(L + L_ROLE + (448 * w + 32 * i) * 32 + lane * 16) = wst[i]; \
    } } while (0)
  const int rb = lane >> 3, cp = lane & 7;
  u64 T64;
  if (__builtin_expect(w < 4, 1)) {
    const u32x4 xr0 = LDB128(rx, (2 * w * DM + 8 * lane) * 2, 0, 0), xr1 = LDB128(rx, ((2 * w + 1) * DM + 8 * lane) * 2, 0, 0);
    u32x4 wqr[4];
#pragma unroll
    for (int i = 0; i < 4; ++i) wqr[i] = LDB128(rwq, ((128 * sp + 32 * w + 8 * i + (lane >> 3)) * (HQ * DH) + kvh * 512 + 64 * b + 8 * (lane & 7)) * 2, 0, WAUX);
    SB(); RAWBAR(); SB();
    float v0[8], v1[8];
#pragma unroll
    for (int i = 0; i < 4; ++i) { v0[2 * i] = bflo(xr0[i]); v0[2 * i + 1] = bfhi(xr0[i]); v1[2 * i] = bflo(xr1[i]); v1[2 * i + 1] = bfhi(xr1[i]); }
    const u32x4 n0 = ln_row(v0), n1 = ln_row(v1);
    *(LAS u32x4*)(L + L_A + 2 * w * A_STR + lane * 16) = n0;
    *(LAS u32x4*)(L + L_A + (2 * w + 1) * A_STR + lane * 16) = n1;
#pragma unroll
    for (int i = 0; i < 4; ++i) *(LAS u32x4*)(L + L_WQ + (32 * w + 8 * i + (lane >> 3)) * 144 + (lane & 7) * 16) = wqr[i];
    STAMP(12);
    wg_barrier();
    STAMP(13);
    const u32x4 a = *(LAS u32x4*)(L + L_A + (lane & 7) * A_STR + (128 * sp + 32 * w + 8 * g) * 2);
#pragma unroll
    for (int nt = 0; nt < 4; ++nt) {
      LAS unsigned char* p = L + L_WQ + (32 * w + 8 * g + ((lane >> 2) & 3)) * 144 + nt * 32 + (lane & 3) * 8;
      const u32x2 lo = tr_read(p), hi = tr_read(p + 4 * 144);
      const f32x4 acc = mfma32(a, u32x4{lo.x, lo.y, hi.x, hi.y}, f32x4{0.f, 0.f, 0.f, 0.f});
      if (lane < 32) {
#pragma unroll
        for (int r = 0; r < 4; ++r) *(LAS float*)(L + L_P + ((w * 8 + 4 * g + r) * 64 + 16 * nt + c16) * 4) = acc[r];
      }
    }
    wg_barrier();
    STAMP(14);
    asm volatile("s_waitcnt lgkmcnt(0)" : "+s"(E));
    T64 = E + 1;
    const u64 T40 = T64 & 0xFFFFFFFFFFull;
#pragma unroll
    for (int i = 0; i < 2; ++i) {
      const int o = tid + 256 * i;
      float sum = 0.f;
#pragma unroll
      for (int ww = 0; ww < 4; ++ww) sum += *(LAS float*)(L + L_P + (ww * 512 + o) * 4);
      const u32 sv = f2u(sum) + 0x80u;
      __hip_atomic_store((GAS u64*)(wsg + WS_QG) + ((kvh * 8 + (o >> 6)) * 4 + sp) * 512 + 64 * b + (o & 63), (T40 << 24) | (u64)(sv >> 8), RLX);
    }
    STAMP(1);
    SB(); RAWBAR(); SB();
    __builtin_amdgcn_s_sleep(QWAIT);
    {
      GAS u64* qgp = (GAS u64*)(wsg + WS_QG) + (kvh * 8 + b) * 4 * 512 + tid;
      u64 qv[8]; int spins = 0;
      for (;;) {
        bool ok = true;
#pragma unroll
        for (int i = 0; i < 8; ++i) qv[i] = __hip_atomic_load(qgp + (i & 3) * 512 + (i >> 2) * 256, RLX);
#pragma unroll
        for (int i = 0; i < 8; ++i) ok &= (qv[i] >> 24) == T40;
        if (__all(ok) || ++spins > SPIN_MAX) break;
        MEMFENCE(); __builtin_amdgcn_s_sleep(2);
      }
      const float qa = (u2f((u32)qv[0] << 8) + u2f((u32)qv[1] << 8)) + (u2f((u32)qv[2] << 8) + u2f((u32)qv[3] << 8));
      const float qb = (u2f((u32)qv[4] << 8) + u2f((u32)qv[5] << 8)) + (u2f((u32)qv[6] << 8) + u2f((u32)qv[7] << 8));
      *(LAS u16*)(L + L_Q + tid * 2) = (u16)cvtpk(qa, qa);
      *(LAS u16*)(L + L_Q + 512 + tid * 2) = (u16)cvtpk(qb, qb);
    }
    STAMP(15);
    wg_barrier();
    SB();
    if (w == 3) {
      const u32x4 sv = {SENT, SENT, SENT, SENT};
      __builtin_amdgcn_raw_buffer_store_b128(sv, rws, WS_PART + (kvh * 32 + j) * 1024 + lane * 16, 0, AUX_SC1);
      if (role == 1) __builtin_amdgcn_raw_buffer_store_b128(sv, rws, WS_Y + ((kvh * 8 + rb) * DM + 64 * b + 8 * cp) * 2, 0, AUX_SC1);
      if (role == 2) __builtin_amdgcn_raw_buffer_store_b64(u32x2{SENT, SENT}, rws, WS_G + (rb * FF + 32 * cgu + 4 * cp) * 2, 0, AUX_SC1);
    }
    if (role == 1 || !LATEW) { u32x4 wst[16]; LOAD_WEIGHTS(wst); STAGE_WEIGHTS(wst); }
    DRAIN();
    wg_barrier();
  } else {
    const int sw = w - 4;
    const int koff = (4 * (c16 & 3) + (c16 >> 2)) * 256 + g * 16, voff = lane * 16;
    const bool vkeep = b >= VB;
    auto tile_so = [&](int t) { const int e = (t - 30) * 4 + sw; return t < 30 ? (t * 16 + sp * 4 + sw) * 4096 : (e < nextra ? (extra0 + e) * 4096 : SK * DH * 2); };
#define ISSUE_K(slot, it) do { const int so_ = tile_so(it); \
    _Pragma("unroll") for (int u = 0; u < 4; ++u) Kb[slot][u] = LDB128(rk, koff + u * 64, so_, KAUX); SB(); } while (0)
#define ISSUE_V(slot, it) do { const int so_ = tile_so(it); \
    _Pragma("unroll") for (int u = 0; u < 4; ++u) Vb[slot][u] = LDB128(rv, voff + u * 1024, so_, VAUX); SB(); } while (0)
#define PACE(it) do { if (TGT) { const u64 due_ = tstart + (u64)((it) * pace); int sp_ = 0; while (__builtin_amdgcn_s_memrealtime() < due_ && ++sp_ < 4096) __builtin_amdgcn_s_sleep(1); } } while (0)
#define ISSUE(slot, it) do { ISSUE_K(slot, it); ISSUE_V(slot, it); } while (0)
    const u64 tstart = TGT ? __builtin_amdgcn_s_memrealtime() : 0ull;
    constexpr int pace = TGT * 4 / 131;
    float mrun = -INFINITY, lrun = 0.f;
    f32x4 O1 = {0.f, 0.f, 0.f, 0.f}, O2 = {0.f, 0.f, 0.f, 0.f};
    auto stream = [&](auto kaux, auto vaux) __attribute__((always_inline)) {
    constexpr int KAUX = decltype(kaux)::value, VAUX = decltype(vaux)::value;
    u32x4 Kb[DEPTH][4], Vb[DEPTH][4];
    RAWBAR();
    if (SLEEP0) __builtin_amdgcn_s_sleep(SLEEP0);
    SB();
    ISSUE(0, 0);
    TSTAMP(12);
    RAWBAR(); SB();
    TSTAMP(13);
    ISSUE_V(1, 1);
    RAWBAR(); SB();
    RAWBAR(); SB();
    ISSUE_K(1, 1);
    asm volatile("s_waitcnt vmcnt(%0)" :: "n"(LAGW) : "memory"); SB();
    TSTAMP(14);
    ISSUE_K(2, 2);
    asm volatile("s_waitcnt vmcnt(%0)" :: "n"(LAGW) : "memory"); SB();
    ISSUE_V(2, 2);
    asm volatile("s_waitcnt vmcnt(%0)" :: "n"(LAGW) : "memory"); SB();
    ISSUE_K(3, 3);
    asm volatile("s_waitcnt vmcnt(%0)" :: "n"(LAGW) : "memory"); SB();
    ISSUE_V(3, 3);
    TSTAMP(15);
    wg_barrier();
    STAMP(2);
    u32x4 Qf[4];
#pragma unroll
    for (int u = 0; u < 4; ++u) Qf[u] = *(LAS u32x4*)(L + L_Q + ((lane & 3) * 128 + 32 * u + 8 * g) * 2);
    const int jx = c16 >> 2;
    const u32 m0k = jx == 0 ? 0x0000FFFFu : jx == 1 ? 0xFFFF0000u : 0u;
    const u32 m1k = jx == 2 ? 0x0000FFFFu : jx == 3 ? 0xFFFF0000u : 0u;
#define PROCESS(slot) do { \
    f32x4 s_ = {0.f, 0.f, 0.f, 0.f}; \
    _Pragma("unroll") for (int u = 0; u < 4; ++u) s_ = mfma32(Kb[slot][u], Qf[u], s_); \
    const float tm_ = xrow_max(fmaxf(fmaxf(s_[0], s_[1]), fmaxf(s_[2], s_[3]))); \
    if (__any(tm_ > mrun)) { \
      const float mn_ = fmaxf(mrun, tm_), al_ = __builtin_amdgcn_exp2f((mrun - mn_) * SC_LOG2); \
      lrun *= al_; mrun = mn_; \
      const float a0_ = dpp<0x00>(al_), a1_ = dpp<0x55>(al_), a2_ = dpp<0xAA>(al_), a3_ = dpp<0xFF>(al_); \
      O1[0] *= a0_; O1[1] *= a1_; O1[2] *= a2_; O1[3] *= a3_; O2[0] *= a0_; O2[1] *= a1_; O2[2] *= a2_; O2[3] *= a3_; \
    } \
    const float mc_ = mrun * SC_LOG2; \
    _Pragma("unroll") for (int i = 0; i < 4; ++i) { \
      const float p_ = __builtin_amdgcn_exp2f(fmaf(s_[i], SC_LOG2, -mc_)); lrun += p_; \
      const u32 pv_ = cvtpk(p_, p_); const u32x2 a_ = {pv_ & m0k, pv_ & m1k}; \
      O1 = mfma16(a_, u32x2{Vb[slot][i].x, Vb[slot][i].y}, O1); O2 = mfma16(a_, u32x2{Vb[slot][i].z, Vb[slot][i].w}, O2); \
    } SB(); } while (0)
#pragma unroll 1
    for (int it = 0; it < 28; it += 4) {
      PROCESS(0); PACE(it + 4); ISSUE(0, it + 4); PROCESS(1); PACE(it + 5); ISSUE(1, it + 5);
      PROCESS(2); PACE(it + 6); ISSUE(2, it + 6); PROCESS(3); PACE(it + 7); ISSUE(3, it + 7);
    }
    PROCESS(0); ISSUE(0, 32); PROCESS(1);
    if (sw < nextra) PROCESS(2);
    if (4 + sw < nextra) PROCESS(3);
    if (8 + sw < nextra) PROCESS(0);
    };
    if (vkeep) stream(ic<0>{}, ic<0>{}); else stream(ic<AUX_NT>{}, ic<AUX_NT>{});
    asm volatile("s_waitcnt lgkmcnt(0)" : "+s"(E));
    T64 = E + 1;
    STAMP(3);
    lrun = xrow_sum(lrun);
#pragma unroll
    for (int h = 0; h < 4; ++h) *(LAS f32x2*)(L + L_MRG + ((sw * 4 + h) * 64 + lane) * 8) = f32x2{O1[h], O2[h]};
    if (lane < 4) { *(LAS float*)(L + L_ST + (lane * 4 + sw) * 4) = mrun; *(LAS float*)(L + L_ST + 64 + (lane * 4 + sw) * 4) = lrun; }
    wg_barrier();
  }
  const u32 T32 = (u32)T64;

  if (w == 1) { if (E1SLEEP) __builtin_amdgcn_s_sleep(E1SLEEP); if (lane == 0) __hip_atomic_store((GAS u64*)(wsg + WS_H1E + (kvh * 32 + j) * FS), T64, RLX); }
  if (w == 0) {
    const int h = g;
    const f32x4 ma = *(LAS f32x4*)(L + L_ST + h * 16), la = *(LAS f32x4*)(L + L_ST + 64 + h * 16);
    const float M = fmaxf(fmaxf(ma[0], ma[1]), fmaxf(ma[2], ma[3]));
    float Ls = 0.f;
    f32x4 oa = {0.f, 0.f, 0.f, 0.f}, ob = {0.f, 0.f, 0.f, 0.f};
#pragma unroll
    for (int ww = 0; ww < 4; ++ww) {
      const float f = __builtin_amdgcn_exp2f((ma[ww] - M) * SC_LOG2);
      Ls = fmaf(f, la[ww], Ls);
      oa += f * *(LAS f32x4*)(L + L_MRG + ((ww * 4 + h) * 64 + 4 * c16) * 8);
      ob += f * *(LAS f32x4*)(L + L_MRG + ((ww * 4 + h) * 64 + 4 * c16) * 8 + 16);
    }
    const float inv = __builtin_amdgcn_rcpf(Ls);
    const u32x4 pk = {nosent(cvtpk(oa[0] * inv, oa[1] * inv)), nosent(cvtpk(oa[2] * inv, oa[3] * inv)), nosent(cvtpk(ob[0] * inv, ob[1] * inv)), nosent(cvtpk(ob[2] * inv, ob[3] * inv))};
    __builtin_amdgcn_raw_buffer_store_b128(pk, rws, WS_PART + (kvh * 32 + j) * 1024 + lane * 16, 0, AUX_SC1);
    const float lam = fmaf(M, SC_LOG2, __builtin_amdgcn_logf(Ls));
    EARLY_DRAIN();
    if (c16 == 0) __hip_atomic_store((GAS u64*)(wsg + WS_H1F + (kvh * 32 + j) * FS) + h, ((u64)T32 << 32) | f2u(lam), RLX);
  }
  STAMP(4);
  if (LATEW && role >= 2) {
    if (tid == 0) *(LAS u32*)(L + L_CNT + 8) = 0u;
    SB(); RAWBAR(); SB();
    if (w < 4) { u32x4 wst[16]; LOAD_WEIGHTS(wst); STAGE_WEIGHTS(wst); }
    wg_barrier();
  }

  if (role == 1) {
    u32x4 Bf[16];
    if (w < 4) {
      const int q = (lane >> 2) & 3;
#pragma unroll
      for (int k = 0; k < 16; ++k) {
        const int d0 = (k >> 2) * 128 + 32 * g + 8 * (q >> 1) + 4 * (q & 1) + (k & 3), d1 = d0 + 16;
        const int sg = (q >> 1) | ((g & 1) << 1);
        const u32x2 lo = tr_read(L + L_ROLE + (d0 ^ (q & 1)) * 128 + 32 * (w ^ sg) + (lane & 3) * 8);
        const u32x2 hi = tr_read(L + L_ROLE + (d1 ^ (q & 1)) * 128 + 32 * (w ^ sg) + (lane & 3) * 8);
        Bf[k] = u32x4{lo.x, lo.y, hi.x, hi.y};
      }
    } else {
#pragma unroll
      for (int k = 0; k < 16; ++k) Bf[k] = u32x4{0u, 0u, 0u, 0u};
    }
    GAS u64* fp = (GAS u64*)(wsg + WS_H1F + (kvh * 32 + 4 * w) * FS) + g;
    const int po = WS_PART + (kvh * 32 + 4 * w) * 1024 + lane * 16;
    LAS unsigned char* pl = L + L_P + w * P_STR + lane * 16;
    LAS unsigned char* al = L + L_P + (lane & 7) * P_STR + g * 16;
    int spins = 0;
    {
      GAS u64* ep = (GAS u64*)(wsg + WS_H1E + (kvh * 32 + 4 * w + (lane & 3)) * FS);
      for (;;) {
        if (__all(__hip_atomic_load(ep, RLX) == T64) || ++spins > SPIN_MAX) break;
        MEMFENCE(); __builtin_amdgcn_s_sleep(1);
      }
    }
    MEMFENCE();
    TSTAMP(12);
    spins = 0;
    for (;;) {
      const u64 f0 = __hip_atomic_load(fp, RLX), f1 = __hip_atomic_load(fp + FS / 8, RLX), f2 = __hip_atomic_load(fp + 2 * FS / 8, RLX), f3 = __hip_atomic_load(fp + 3 * FS / 8, RLX);
      u32x4 pv[4];
#pragma unroll
      for (int i = 0; i < 4; ++i) pv[i] = LDB128(rws, po + i * 1024, 0, AUX_SC1);
      SB();
      const float l0 = u2f((u32)f0), l1 = u2f((u32)f1), l2 = u2f((u32)f2), l3 = u2f((u32)f3);
      const float lm = fmaxf(fmaxf(l0, l1), fmaxf(l2, l3));
      float e[4] = {__builtin_amdgcn_exp2f(l0 - lm), __builtin_amdgcn_exp2f(l1 - lm), __builtin_amdgcn_exp2f(l2 - lm), __builtin_amdgcn_exp2f(l3 - lm)};
      const float inv = __builtin_amdgcn_rcpf((e[0] + e[1]) + (e[2] + e[3]));
#pragma unroll
      for (int i = 0; i < 4; ++i) e[i] *= inv;
      SB();
      u32 m = 0; f32x2 o[4];
#pragma unroll
      for (int d = 0; d < 4; ++d) o[d] = f32x2{0.f, 0.f};
#pragma unroll
      for (int i = 0; i < 4; ++i) {
        m = mx4(m, pv[i]);
#pragma unroll
        for (int d = 0; d < 4; ++d) o[d] += e[i] * f32x2{bflo(pv[i][d]), bfhi(pv[i][d])};
      }
      *(LAS u32x4*)pl = u32x4{cvtpk(o[0][0], o[0][1]), cvtpk(o[1][0], o[1][1]), cvtpk(o[2][0], o[2][1]), cvtpk(o[3][0], o[3][1])};
      const bool ok = (m != SENT) & ((u32)(f0 >> 32) == T32) & ((u32)(f1 >> 32) == T32) & ((u32)(f2 >> 32) == T32) & ((u32)(f3 >> 32) == T32);
      if (__all(ok) || ++spins > SPIN_MAX) break;
      MEMFENCE();
    }
    RAWSTAMP(10, spins + 1);
    TSTAMP(13);
    wg_barrier();
    if (w == 1 && E2EARLY) { if (E2SLEEP) __builtin_amdgcn_s_sleep(E2SLEEP); if (lane < 8) __hip_atomic_store((GAS u64*)(wsg + WS_H2F + (lane * 64 + kvh * 8 + b) * FS), T64, RLX); }
    TSTAMP(14);
    if (w < 4) {
      u32x4 av[16];
#pragma unroll
      for (int k = 0; k < 16; ++k) av[k] = *(LAS u32x4*)(al + k * 64);
      SB();
      f32x4 a0 = {0.f, 0.f, 0.f, 0.f}, a1 = {0.f, 0.f, 0.f, 0.f};
#pragma unroll
      for (int k = 0; k < 16; k += 2) { a0 = mfma32(av[k], Bf[k], a0); a1 = mfma32(av[k + 1], Bf[k + 1], a1); }
      const float yv4[4] = {a0[0] + a1[0], a0[1] + a1[1], a0[2] + a1[2], a0[3] + a1[3]};
      const bool odd = lane & 1;
      const u32x2 pk = pack_pairs(yv4, odd);
      const int yo = WS_Y + ((kvh * 8 + 4 * g + (odd ? 2 : 0)) * DM + 64 * b + 16 * w + (c16 & 14)) * 2;
      if (lane < 32) { __builtin_amdgcn_raw_buffer_store_b32(pk.x, rws, yo, 0, AUX_SC1); __builtin_amdgcn_raw_buffer_store_b32(pk.y, rws, yo + DM * 2, 0, AUX_SC1); }
    }
    if (!E2EARLY) { DRAIN(); wg_barrier(); if (w == 0 && lane < 8) __hip_atomic_store((GAS u64*)(wsg + WS_H2F + (lane * 64 + kvh * 8 + b) * FS), T64, RLX); }
    STAMP(5);
    STAMP(6);
  } else if (role == 2) {
    u32x4 Bf[16];
    if (w < 4) {
      const int q = (lane >> 2) & 3, p = lane & 3;
#pragma unroll
      for (int k = 0; k < 16; ++k) {
        LAS unsigned char* pp = L + L_ROLE + (p >> 1) * 32768 + (32 * k + 8 * g + q) * 64 + 32 * ((w >> 1) ^ (g & 1)) + 16 * (w & 1) + 8 * (p & 1);
        const u32x2 lo = tr_read(pp), hi = tr_read(pp + 256);
        Bf[k] = u32x4{lo.x, lo.y, hi.x, hi.y};
      }
    } else {
#pragma unroll
      for (int k = 0; k < 16; ++k) Bf[k] = u32x4{0u, 0u, 0u, 0u};
    }
    const u32x4 xv = LDB128(rx, (w * DM + 8 * lane) * 2, 0, 0);
    float x8[8];
#pragma unroll
    for (int i = 0; i < 4; ++i) { x8[2 * i] = bflo(xv[i]); x8[2 * i + 1] = bfhi(xv[i]); }
    const int yo = WS_Y + (w * DM + 8 * lane) * 2;
    LAS unsigned char* aw = L + L_A + w * A_STR + lane * 16;
    LAS unsigned char* ar = L + L_A + (lane & 7) * A_STR + 8 * g * 2;
    u32x4 yv[8];
    {
      GAS u64* fp = (GAS u64*)(wsg + WS_H2F + (kvh * 64 + lane) * FS);
      LAS u32* mk = (LAS u32*)(L + L_CNT + 8);
      u32 done = 0; int spins = 0;
#pragma unroll
      for (int i = 0; i < 8; ++i) yv[i] = u32x4{0u, 0u, 0u, 0u};
      for (;;) {
        u32 rdy;
        if (w == 0) {
          const u64 bal = __builtin_amdgcn_ballot_w64(__hip_atomic_load(fp, RLX) == T64);
          rdy = 0;
#pragma unroll
          for (int i = 0; i < 8; ++i) rdy |= (((bal >> (8 * i)) & 0xFFull) == 0xFFull ? 1u : 0u) << i;
          if (rdy != done && lane == 0) __hip_atomic_store(mk, rdy, __ATOMIC_RELAXED, __HIP_MEMORY_SCOPE_WORKGROUP);
        } else {
          rdy = __builtin_amdgcn_readfirstlane(__hip_atomic_load(mk, __ATOMIC_RELAXED, __HIP_MEMORY_SCOPE_WORKGROUP));
        }
#pragma unroll
        for (int i = 0; i < 8; ++i)
          if (((rdy & ~done) >> i) & 1u) yv[i] = LDB128(rws, yo + i * (8 * DM * 2), 0, AUX_SC1);
        done |= rdy;
        if (done == 0xFFu || ++spins > SPIN_MAX * 8) break;
        MEMFENCE(); if (w == 0) { if (PSLEEP) __builtin_amdgcn_s_sleep(PSLEEP); } else __builtin_amdgcn_s_sleep(LSLEEP);
      }
    }
    STAMP(7);
    {
      int spins = 0;
      for (;;) {
        u32 bad = 0;
#pragma unroll
        for (int i = 0; i < 8; ++i) if (!__all(mx4(0u, yv[i]) != SENT)) bad |= 1u << i;
        if (bad == 0 || ++spins > SPIN_MAX) break;
        MEMFENCE();
#pragma unroll
        for (int i = 0; i < 8; ++i) if ((bad >> i) & 1u) yv[i] = LDB128(rws, yo + i * (8 * DM * 2), 0, AUX_SC1);
        RAWSTAMP(11, spins + 1);
      }
    }
    float v[8];
#pragma unroll
    for (int e = 0; e < 8; ++e) v[e] = x8[e];
#pragma unroll
    for (int i = 0; i < 8; ++i) {
#pragma unroll
      for (int e = 0; e < 4; ++e) { v[2 * e] += bflo(yv[i][e]); v[2 * e + 1] += bfhi(yv[i][e]); }
    }
    TSTAMP(13);
    *(LAS u32x4*)aw = ln_row(v);
    TSTAMP(14);
    wg_barrier();
    if (w == 1 && E3EARLY) { if (E3SLEEP) __builtin_amdgcn_s_sleep(E3SLEEP); if (lane < 8) __hip_atomic_store((GAS u64*)(wsg + WS_H3F + (lane * 64 + cgu) * FS), T64, RLX); }
    STAMP(8);
    if (w < 4) {
      u32x4 av[16];
#pragma unroll
      for (int k = 0; k < 16; ++k) av[k] = *(LAS u32x4*)(ar + k * 64);
      SB();
      f32x4 a0 = {0.f, 0.f, 0.f, 0.f}, a1 = {0.f, 0.f, 0.f, 0.f};
#pragma unroll
      for (int k = 0; k < 16; k += 2) { a0 = mfma32(av[k], Bf[k], a0); a1 = mfma32(av[k + 1], Bf[k + 1], a1); }
      float r4[4];
#pragma unroll
      for (int e = 0; e < 4; ++e) {
        const float z = a0[e] + a1[e];
        const float up = dpp<0x128>(z);
        const float t = z * fmaf(z * z, 0.044715f * 0.7978845608028654f, 0.7978845608028654f);
        const float sg = __builtin_amdgcn_rcpf(1.0f + __builtin_amdgcn_exp2f(t * (-2.0f * 1.4426950408889634f)));
        r4[e] = z * sg * up;
      }
      const bool odd = lane & 1;
      const u32x2 pk = pack_pairs(r4, odd);
      const int gof = WS_G + ((4 * g + (odd ? 2 : 0)) * FF + 32 * cgu + 8 * w + (c16 & 6)) * 2;
      if (lane < 32 && c16 < 8) { __builtin_amdgcn_raw_buffer_store_b32(pk.x, rws, gof, 0, AUX_SC1); __builtin_amdgcn_raw_buffer_store_b32(pk.y, rws, gof + FF * 2, 0, AUX_SC1); }
    }
    if (!E3EARLY) { DRAIN(); wg_barrier(); if (w == 0 && lane < 8) __hip_atomic_store((GAS u64*)(wsg + WS_H3F + (lane * 64 + cgu) * FS), T64, RLX); }
    STAMP(9);
  } else if (role == 3) {
    u32x4 Bf[7];
#pragma unroll
    for (int u = 0; u < 7; ++u) Bf[u] = bfrag(L + L_ROLE + (224 * w + 32 * u) * 32 + trl, 128);
    int go[4]; LAS unsigned char* gl[4];
#pragma unroll
    for (int i = 0; i < 4; ++i) {
      const int ci = (w + 8 * i) * 64 + lane, row = ci / 224; const bool in = i < 3 || w < 4;
      go[i] = in ? WS_G + ci * 16 : WS_END;
      gl[i] = L + L_P + (in ? row * G_STR + (ci - row * 224) * 16 : 8 * G_STR + lane * 16);
    }
    LAS unsigned char* ar = L + L_P + (lane & 7) * G_STR + (7 * w * 32 + 8 * g) * 2;
    LAS unsigned char* rw = L + L_RED + ((w * 8 + 4 * g) * 16 + c16) * 4;
    f32x2 hres = {0.f, 0.f};
    if (w == 0) {
      const u32 xw = *(const GAS u32*)((const GAS unsigned char*)x + (rb * DM + 16 * cdn + 2 * cp) * 2);
      GAS u64* fp = (GAS u64*)(wsg + WS_H2F + (kvh * 64 + (lane & 7) * 8 + (cdn >> 2)) * FS); int spins = 0;
      for (;;) { const u64 f0 = __hip_atomic_load(fp, RLX); if (__all(f0 == T64) || ++spins > SPIN_MAX) break; MEMFENCE(); __builtin_amdgcn_s_sleep(2); }
      MEMFENCE();
      for (spins = 0;; ) {
        u32 yy[8], m = 0;
#pragma unroll
        for (int q = 0; q < 8; ++q) yy[q] = __builtin_amdgcn_raw_buffer_load_b32(rws, WS_Y + ((q * 8 + rb) * DM + 16 * cdn + 2 * cp) * 2, 0, AUX_SC1);
        hres = f32x2{bflo(xw), bfhi(xw)};
#pragma unroll
        for (int q = 0; q < 8; ++q) { m = umax(m, yy[q]); hres += f32x2{bflo(yy[q]), bfhi(yy[q])}; }
        if (__all(m != SENT) || ++spins > SPIN_MAX) break;
        MEMFENCE();
      }
      GAS u64* gp = (GAS u64*)(wsg + WS_H3F + (kvh * 64 + lane) * FS); spins = 0;
      for (;;) {
        const u64 f0 = lane < NGU ? __hip_atomic_load(gp, RLX) : T64;
        if (__all(f0 == T64) || ++spins > SPIN_MAX) break;
        MEMFENCE(); if (PSLEEP) __builtin_amdgcn_s_sleep(PSLEEP);
      }
      if (cdn == 0 && lane == 0) __hip_atomic_store((GAS u64*)(wsg + WS_E), T64, RLX);
    }
    wg_barrier();
    STAMP(9);
    for (int spins = 0;; ) {
      u32x4 gv[4];
#pragma unroll
      for (int i = 0; i < 4; ++i) gv[i] = LDB128(rws, go[i], 0, AUX_SC1);
      SB();
      u32 m = 0;
#pragma unroll
      for (int i = 0; i < 4; ++i) { m = mx4(m, gv[i]); *(LAS u32x4*)gl[i] = gv[i]; }
      if (__all(m != SENT) || ++spins > SPIN_MAX) break;
      RAWSTAMP(11, spins + 1);
      MEMFENCE();
    }
    TSTAMP(12);
    wg_barrier();
    TSTAMP(13);
    {
      u32x4 av[7];
#pragma unroll
      for (int u = 0; u < 7; ++u) av[u] = *(LAS u32x4*)(ar + u * 64);
      SB();
      f32x4 a0 = {0.f, 0.f, 0.f, 0.f}, a1 = {0.f, 0.f, 0.f, 0.f};
#pragma unroll
      for (int u = 0; u < 7; ++u) { if (u & 1) a1 = mfma32(av[u], Bf[u], a1); else a0 = mfma32(av[u], Bf[u], a0); }
      if (lane < 32) {
#pragma unroll
        for (int r = 0; r < 4; ++r) *(LAS float*)(rw + r * 64) = a0[r] + a1[r];
      }
    }
    TSTAMP(14);
    wg_barrier();
    TSTAMP(15);
    if (w == 0) {
      f32x2 rr[8];
#pragma unroll
      for (int ww = 0; ww < 8; ++ww) rr[ww] = *(LAS f32x2*)(L + L_RED + ((ww * 8 + rb) * 16 + 2 * cp) * 4);
      const f32x2 sm = hres + (((rr[0] + rr[1]) + (rr[2] + rr[3])) + ((rr[4] + rr[5]) + (rr[6] + rr[7])));
      *(GAS u32*)((GAS unsigned char*)out + (rb * DM + 16 * cdn + 2 * cp) * 2) = cvtpk(sm[0], sm[1]);
    }
    STAMP(8);
  }
  STAMP_FLUSH();
}

extern "C" void kernel_launch(void* const* d_in, const int* in_sizes, int n_in, void* d_out, int out_size, void* d_ws, size_t ws_size, hipStream_t stream) {
  fused_decode_block<<<NWG, NTHR, 0, stream>>>((const u16*)d_in[0], (const u16*)d_in[1], (const u16*)d_in[2], (const u16*)d_in[3], (const u16*)d_in[4],
                                              (const u16*)d_in[6], (const u16*)d_in[7], (const u16*)d_in[8], (u16*)d_out, (unsigned char*)d_ws);
}
```

```cpp
#include <hip/hip_runtime.h>
#include <hip/hip_bf16.h>
#include <stdint.h>

typedef unsigned short u16;
typedef unsigned u32;
typedef unsigned long long u64;
typedef u32 u32x4 __attribute__((ext_vector_type(4)));
typedef u32 u32x2 __attribute__((ext_vector_type(2)));
typedef float f32x4 __attribute__((ext_vector_type(4)));
typedef float f32x2 __attribute__((ext_vector_type(2)));
typedef short s16x8 __attribute__((ext_vector_type(8)));
typedef short s16x4 __attribute__((ext_vector_type(4)));
typedef __bf16 bf16x8_t __attribute__((ext_vector_type(8)));
#define GAS __attribute__((address_space(1)))
#define LAS __attribute__((address_space(3)))
#define RLX __ATOMIC_RELAXED, __HIP_MEMORY_SCOPE_AGENT

constexpr int NB = 8, DM = 512, HQ = 32, HKV = 8, SK = 8192, DH = 128, FF = 1792;
constexpr int NWG = 256, NTHR = 512;
constexpr int NGU = FF / 32;
constexpr int GUD = 4;
constexpr int SLEEP0 = 8;
constexpr int QWAIT = 30;
constexpr int E1SLEEP = 4, E2SLEEP = 3, E3SLEEP = 3;
constexpr int TGT = 3800;
constexpr float SC_LOG2 = 0.08838834764831845f * 1.4426950408889634f;
constexpr float LN_EPS = 1e-5f;
constexpr int AUX_NT = 2, AUX_SC1 = 16;
constexpr int SPIN_MAX = 1 << 16;

constexpr int FS = 256;
constexpr int WS_E    = 0;
constexpr int WS_H1F  = 4096;
constexpr int WS_H1E  = WS_H1F + 256 * FS;
constexpr int WS_H2F  = WS_H1E + 256 * FS;
constexpr int WS_H3F  = WS_H2F + 512 * FS;
constexpr int WS_QG   = WS_H3F + 512 * FS;
constexpr int WS_PART = WS_QG + HKV * NB * 4 * 512 * 8;
constexpr int WS_Y    = WS_PART + HKV * 32 * 4 * 128 * 2;
constexpr int WS_G    = WS_Y + HKV * NB * DM * 2;
constexpr int WS_END  = WS_G + NB * FF * 2;

constexpr int L_ROLE = 0;
constexpr int L_WQ = 0;
constexpr int L_MRG = 65536;
constexpr int A_STR = 1040;
constexpr int L_A = L_MRG + 16384;
constexpr int L_RED = L_A + 8 * A_STR;
constexpr int L_Q = L_RED + 8192;
constexpr int L_ST = L_Q + 1024;
constexpr int L_CNT = L_ST + 192;
constexpr int P_STR = 1040;
constexpr int L_P = L_ST + 768;
constexpr int G_STR = 3600;
constexpr int LDS_BYTES = L_P + 32 * P_STR;

typedef __bf16 bf16x2_t __attribute__((ext_vector_type(2)));
__device__ __forceinline__ u32 cvtpk(float lo, float hi) { const bf16x2_t r = {(__bf16)lo, (__bf16)hi}; return __builtin_bit_cast(u32, r); }
__device__ __forceinline__ float u2f(u32 v) { return __builtin_bit_cast(float, v); }
__device__ __forceinline__ u32 f2u(float v) { return __builtin_bit_cast(u32, v); }
__device__ __forceinline__ float bflo(u32 v) { return __builtin_bit_cast(float, v << 16); }
__device__ __forceinline__ float bfhi(u32 v) { return __builtin_bit_cast(float, v & 0xffff0000u); }
template <int CTRL> __device__ __forceinline__ float dpp(float x) { return __builtin_bit_cast(float, __builtin_amdgcn_mov_dpp(__builtin_bit_cast(int, x), CTRL, 0xf, 0xf, true)); }
__device__ __forceinline__ float xrow_max(float x) {
  auto s = __builtin_amdgcn_permlane16_swap(f2u(x), f2u(x), false, false);
  x = fmaxf(u2f(s[0]), u2f(s[1]));
  auto t = __builtin_amdgcn_permlane32_swap(f2u(x), f2u(x), false, false);
  return fmaxf(u2f(t[0]), u2f(t[1]));
}
__device__ __forceinline__ float xrow_sum(float x) {
  auto s = __builtin_amdgcn_permlane16_swap(f2u(x), f2u(x), false, false);
  x = u2f(s[0]) + u2f(s[1]);
  auto t = __builtin_amdgcn_permlane32_swap(f2u(x), f2u(x), false, false);
  return u2f(t[0]) + u2f(t[1]);
}
__device__ __forceinline__ f32x4 mfma32(u32x4 a, u32x4 b, f32x4 c) {
  return __builtin_amdgcn_mfma_f32_16x16x32_bf16(__builtin_bit_cast(bf16x8_t, a), __builtin_bit_cast(bf16x8_t, b), c, 0, 0, 0);
}
__device__ __forceinline__ f32x4 mfma16(u32x2 a, u32x2 b, f32x4 c) {
  return __builtin_amdgcn_mfma_f32_16x16x16bf16_1k(__builtin_bit_cast(s16x4, a), __builtin_bit_cast(s16x4, b), c, 0, 0, 0);
}
typedef short v4i16_t __attribute__((ext_vector_type(4)));
__device__ __forceinline__ u32x2 tr_read(LAS unsigned char* p) {
  return __builtin_bit_cast(u32x2, __builtin_amdgcn_ds_read_tr16_b64_v4i16((LAS v4i16_t*)p));
}
__device__ __forceinline__ u32x4 bfrag(LAS unsigned char* p, int second_off) {
  const u32x2 a = tr_read(p), b = tr_read(p + second_off);
  return u32x4{a.x, a.y, b.x, b.y};
}
constexpr u32 SENT = 0xFFFFFFFFu;
__device__ __forceinline__ u32 nosent(u32 d) { return d == SENT ? 0x7FFF7FFFu : d; }
__device__ __forceinline__ u32 umax(u32 a, u32 b) { return a > b ? a : b; }
__device__ __forceinline__ u32 mx4(u32 m, u32x4 v) { return umax(umax(umax(m, v.x), v.y), umax(v.z, v.w)); }
__device__ __forceinline__ u32x2 pack_pairs(const float (&a)[4], bool odd) {
  const float n0 = dpp<0xB1>(odd ? a[0] : a[2]), n1 = dpp<0xB1>(odd ? a[1] : a[3]);
  const float o0 = odd ? a[2] : a[0], o1 = odd ? a[3] : a[1];
  return u32x2{nosent(cvtpk(odd ? n0 : o0, odd ? o0 : n0)), nosent(cvtpk(odd ? n1 : o1, odd ? o1 : n1))};
}
__device__ __forceinline__ __amdgpu_buffer_rsrc_t mkrs(const void* p, u32 bytes) { return __builtin_amdgcn_make_buffer_rsrc((void*)p, 0, bytes, 0x00020000); }
#define LDB128(rs, vo, so, aux) __builtin_amdgcn_raw_buffer_load_b128(rs, vo, so, aux)
#define SB() __builtin_amdgcn_sched_barrier(0)
#define DRAIN() asm volatile("s_waitcnt vmcnt(0)" ::: "memory")
#define MEMFENCE() asm volatile("" ::: "memory")
#define RAWBAR() asm volatile("s_barrier" ::: "memory")
__device__ __forceinline__ void wg_barrier() { asm volatile("s_waitcnt lgkmcnt(0)\n\ts_barrier" ::: "memory"); }

__device__ __forceinline__ u32x4 ln_row(const float (&v)[8]) {
  const float piv = u2f(__builtin_amdgcn_readfirstlane(f2u(v[0])));
  float d[8], s = 0.f, q = 0.f;
#pragma unroll
  for (int i = 0; i < 8; ++i) { d[i] = v[i] - piv; s += d[i]; q = fmaf(d[i], d[i], q); }
  s += dpp<0xB1>(s); q += dpp<0xB1>(q); s += dpp<0x4E>(s); q += dpp<0x4E>(q); s += dpp<0x141>(s); q += dpp<0x141>(q); s += dpp<0x140>(s); q += dpp<0x140>(q);
  s = xrow_sum(s); q = xrow_sum(q);
  const float mean = s * (1.0f / DM);
  const float var = fmaxf(fmaf(-mean, mean, q * (1.0f / DM)), 0.f);
  const float rs = __builtin_amdgcn_rsqf(var + LN_EPS), off = -mean * rs;
  return u32x4{cvtpk(fmaf(d[0], rs, off), fmaf(d[1], rs, off)), cvtpk(fmaf(d[2], rs, off), fmaf(d[3], rs, off)), cvtpk(fmaf(d[4], rs, off), fmaf(d[5], rs, off)), cvtpk(fmaf(d[6], rs, off), fmaf(d[7], rs, off))};
}

__global__ void __launch_bounds__(NTHR, 2)
fused_decode_block(const u16* x_, const u16* Kc_, const u16* Vc_, const u16* wq_, const u16* wo_,
                   const u16* wg_, const u16* wu_, const u16* wd_, u16* out_, unsigned char* ws_) {
  const u16 *x = x_, *Kc = Kc_, *Vc = Vc_, *wq = wq_, *wo = wo_, *wg = wg_, *wu = wu_, *wd = wd_; u16* out = out_; unsigned char* ws = ws_;
  asm volatile("" : "+s"(x), "+s"(Kc), "+s"(Vc), "+s"(wq), "+s"(wo), "+s"(wg), "+s"(wu), "+s"(wd), "+s"(out), "+s"(ws));
  __shared__ __attribute__((aligned(16))) unsigned char lds_raw[LDS_BYTES];
  LAS unsigned char* L = (LAS unsigned char*)lds_raw;
  const int tid = threadIdx.x, lane = tid & 63, w = __builtin_amdgcn_readfirstlane(tid >> 6);
  const int blk = blockIdx.x, kvh = blk & 7, j = blk >> 3, b = j >> 2, sp = j & 3;
  const int g = lane >> 4, c16 = lane & 15;
  GAS unsigned char* wsg = (GAS unsigned char*)ws;

#define LOAD_EPOCH(E_) asm volatile("s_load_dwordx2 %0, %1, 0x0" : "=s"(E_) : "s"(ws))
  const __amdgpu_buffer_rsrc_t rx = mkrs(x, NB * DM * 2);
  const __amdgpu_buffer_rsrc_t rwq = mkrs(wq, DM * HQ * DH * 2);
  const int xt = b < 7 ? 130 + GUD / 4 : 130;
  const int nt0 = xt - 8, nt1 = b < 7 ? xt - GUD : xt;
  const int nextra = (sp == 0 ? nt0 : (sp == 1 ? nt1 : xt)) - 120;
  const int extra0 = 480 + (sp == 0 ? 0 : (sp == 1 ? nt0 - 120 : nt0 + nt1 - 240 + (sp - 2) * (xt - 120)));
  const size_t kvbase = (size_t)(b * HKV + kvh) * SK * DH;
  const __amdgpu_buffer_rsrc_t rk = mkrs(Kc + kvbase, SK * DH * 2);
  const __amdgpu_buffer_rsrc_t rv = mkrs(Vc + kvbase, SK * DH * 2);
  const __amdgpu_buffer_rsrc_t rws = mkrs(ws, WS_END);

  const int trl = (8 * g + ((lane >> 2) & 3)) * 32 + (lane & 3) * 8;
  const int role = sp == 0 ? 1 : sp == 1 ? (b < 7 ? 2 : 3) : (sp == 2 && b < 3 ? 3 : 0);
  const int cgu = kvh * 7 + b, cdn = kvh * 4 + (sp == 1 ? 0 : 1 + b);
#define LOAD_WEIGHTS(wst) do { \
      const u16* p0 = role == 1 ? wo : (role == 2 ? wg : wd); \
      const u16* p1 = role == 2 ? wu : p0; \
      const u32 n0b = role == 0 ? 0u : (role == 1 ? (u32)(HQ * DH * DM * 2) : (u32)(DM * FF * 2)); \
      const u32 n1b = role == 3 ? 0u : n0b; \
      const __amdgpu_buffer_rsrc_t r0 = mkrs(p0, n0b), r1 = mkrs(p1, n0b), r2 = mkrs(p1, n1b); \
      const int v0o = role == 1 ? ((kvh * 512 + 128 * w + (lane >> 3)) * DM + 64 * b + 8 * (lane & 7)) * 2 \
                    : role == 2 ? ((128 * w + (lane >> 2)) * FF + 32 * cgu + 8 * (lane & 3)) * 2 \
                                : ((448 * w + (lane >> 1)) * DM + 16 * cdn + 8 * (lane & 1)) * 2; \
      const int st = role == 1 ? 8 * DM * 2 : role == 2 ? 16 * FF * 2 : 32 * DM * 2; \
      const int v8o = role == 2 ? v0o : v0o + 8 * st; \
      _Pragma("unroll") for (int i = 0; i < 8; ++i) wst[i] = LDB128(r0, v0o + i * st, 0, AUX_NT); \
      _Pragma("unroll") for (int i = 0; i < 6; ++i) wst[8 + i] = LDB128(r1, v8o + i * st, 0, AUX_NT); \
      _Pragma("unroll") for (int i = 6; i < 8; ++i) wst[8 + i] = LDB128(r2, v8o + i * st, 0, AUX_NT); \
      SB(); } while (0)
#define STAGE_WEIGHTS(wst) do { \
    if (role == 1) { \
      _Pragma("unroll") for (int i = 0; i < 16; ++i) { const int r = 128 * w + 8 * i + (lane >> 3), ch = lane & 7; \
        *(LAS u32x4*)(L + L_ROLE + (r ^ ((r >> 2) & 1)) * 128 + 32 * ((ch >> 1) ^ (((r >> 3) & 1) | (((r >> 5) & 1) << 1))) + 16 * (ch & 1)) = wst[i]; } \
    } else if (role == 2) { \
      _Pragma("unroll") for (int i = 0; i < 16; ++i) { const int r = 128 * w + 16 * (i & 7) + (lane >> 2), ch = lane & 3; \
        *(LAS u32x4*)(L + L_ROLE + (i >> 3) * 32768 + r * 64 + 32 * ((ch >> 1) ^ ((r >> 3) & 1)) + 16 * (ch & 1)) = wst[i]; } \
    } else if (role == 3) { \
      _Pragma("unroll") for (int i = 0; i < 14; ++i) *(LAS u32x4*)(L + L_ROLE + (448 * w + 32 * i) * 32 + lane * 16) = wst[i]; \
    } } while (0)
  const int rb = lane >> 3, cp = lane & 7;
  u64 T64;
  if (__builtin_expect(w < 4, 1)) {
    const u32x4 xr0 = LDB128(rx, (2 * w * DM + 8 * lane) * 2, 0, 0), xr1 = LDB128(rx, ((2 * w + 1) * DM + 8 * lane) * 2, 0, 0);
    u32x4 wqr[4];
#pragma unroll
    for (int i = 0; i < 4; ++i) wqr[i] = LDB128(rwq, ((128 * sp + 32 * w + 8 * i + (lane >> 3)) * (HQ * DH) + kvh * 512 + 64 * b + 8 * (lane & 7)) * 2, 0, AUX_NT);
    SB();
    u64 E; LOAD_EPOCH(E);
    SB(); RAWBAR(); SB();
    float v0[8], v1[8];
#pragma unroll
    for (int i = 0; i < 4; ++i) { v0[2 * i] = bflo(xr0[i]); v0[2 * i + 1] = bfhi(xr0[i]); v1[2 * i] = bflo(xr1[i]); v1[2 * i + 1] = bfhi(xr1[i]); }
    const u32x4 n0 = ln_row(v0), n1 = ln_row(v1);
    *(LAS u32x4*)(L + L_A + 2 * w * A_STR + lane * 16) = n0;
    *(LAS u32x4*)(L + L_A + (2 * w + 1) * A_STR + lane * 16) = n1;
#pragma unroll
    for (int i = 0; i < 4; ++i) *(LAS u32x4*)(L + L_WQ + (32 * w + 8 * i + (lane >> 3)) * 144 + (lane & 7) * 16) = wqr[i];
    wg_barrier();
    const u32x4 a = *(LAS u32x4*)(L + L_A + (lane & 7) * A_STR + (128 * sp + 32 * w + 8 * g) * 2);
#pragma unroll
    for (int nt = 0; nt < 4; ++nt) {
      LAS unsigned char* p = L + L_WQ + (32 * w + 8 * g + ((lane >> 2) & 3)) * 144 + nt * 32 + (lane & 3) * 8;
      const u32x2 lo = tr_read(p), hi = tr_read(p + 4 * 144);
      const f32x4 acc = mfma32(a, u32x4{lo.x, lo.y, hi.x, hi.y}, f32x4{0.f, 0.f, 0.f, 0.f});
      if (lane < 32) {
#pragma unroll
        for (int r = 0; r < 4; ++r) *(LAS float*)(L + L_P + ((w * 8 + 4 * g + r) * 64 + 16 * nt + c16) * 4) = acc[r];
      }
    }
    wg_barrier();
    asm volatile("s_waitcnt lgkmcnt(0)" : "+s"(E));
    T64 = E + 1;
    const u64 T40 = T64 & 0xFFFFFFFFFFull;
#pragma unroll
    for (int i = 0; i < 2; ++i) {
      const int o = tid + 256 * i;
      float sum = 0.f;
#pragma unroll
      for (int ww = 0; ww < 4; ++ww) sum += *(LAS float*)(L + L_P + (ww * 512 + o) * 4);
      const u32 sv = f2u(sum) + 0x80u;
      __hip_atomic_store((GAS u64*)(wsg + WS_QG) + ((kvh * 8 + (o >> 6)) * 4 + sp) * 512 + 64 * b + (o & 63), (T40 << 24) | (u64)(sv >> 8), RLX);
    }
    SB(); RAWBAR(); SB();
    __builtin_amdgcn_s_sleep(QWAIT);
    {
      GAS u64* qgp = (GAS u64*)(wsg + WS_QG) + (kvh * 8 + b) * 4 * 512 + tid;
      u64 qv[8]; int spins = 0;
      for (;;) {
        bool ok = true;
#pragma unroll
        for (int i = 0; i < 8; ++i) qv[i] = __hip_atomic_load(qgp + (i & 3) * 512 + (i >> 2) * 256, RLX);
#pragma unroll
        for (int i = 0; i < 8; ++i) ok &= (qv[i] >> 24) == T40;
        if (__all(ok) || ++spins > SPIN_MAX) break;
        MEMFENCE(); __builtin_amdgcn_s_sleep(2);
      }
      const float qa = (u2f((u32)qv[0] << 8) + u2f((u32)qv[1] << 8)) + (u2f((u32)qv[2] << 8) + u2f((u32)qv[3] << 8));
      const float qb = (u2f((u32)qv[4] << 8) + u2f((u32)qv[5] << 8)) + (u2f((u32)qv[6] << 8) + u2f((u32)qv[7] << 8));
      *(LAS u16*)(L + L_Q + tid * 2) = (u16)cvtpk(qa, qa);
      *(LAS u16*)(L + L_Q + 512 + tid * 2) = (u16)cvtpk(qb, qb);
    }
    wg_barrier();
    SB();
    if (w == 3) {
      const u32x4 sv = {SENT, SENT, SENT, SENT};
      __builtin_amdgcn_raw_buffer_store_b128(sv, rws, WS_PART + (kvh * 32 + j) * 1024 + lane * 16, 0, AUX_SC1);
      if (role == 1) __builtin_amdgcn_raw_buffer_store_b128(sv, rws, WS_Y + ((kvh * 8 + rb) * DM + 64 * b + 8 * cp) * 2, 0, AUX_SC1);
      if (role == 2) __builtin_amdgcn_raw_buffer_store_b64(u32x2{SENT, SENT}, rws, WS_G + (rb * FF + 32 * cgu + 4 * cp) * 2, 0, AUX_SC1);
    }
    if (role == 1) { u32x4 wst[16]; LOAD_WEIGHTS(wst); STAGE_WEIGHTS(wst); }
    DRAIN();
    wg_barrier();
  } else {
    const int sw = w - 4;
    const int koff = (4 * (c16 & 3) + (c16 >> 2)) * 256 + g * 16, voff = lane * 16;
    auto tile_so = [&](int t) { const int e = (t - 30) * 4 + sw; return t < 30 ? (t * 16 + sp * 4 + sw) * 4096 : (e < nextra ? (extra0 + e) * 4096 : SK * DH * 2); };
#define ISSUE_K(slot, it) do { const int so_ = tile_so(it); \
    _Pragma("unroll") for (int u = 0; u < 4; ++u) Kb[slot][u] = LDB128(rk, koff + u * 64, so_, AUX_NT); SB(); } while (0)
#define ISSUE_V(slot, it) do { const int so_ = tile_so(it); \
    _Pragma("unroll") for (int u = 0; u < 4; ++u) Vb[slot][u] = LDB128(rv, voff + u * 1024, so_, AUX_NT); SB(); } while (0)
#define PACE(it) do { const u64 due_ = tstart + (u64)((it) * pace); int sp_ = 0; while (__builtin_amdgcn_s_memrealtime() < due_ && ++sp_ < 4096) __builtin_amdgcn_s_sleep(1); } while (0)
#define ISSUE(slot, it) do { ISSUE_K(slot, it); ISSUE_V(slot, it); } while (0)
    u64 E; LOAD_EPOCH(E);
    const u64 tstart = __builtin_amdgcn_s_memrealtime();
    constexpr int pace = TGT * 4 / 131;
    float mrun = -INFINITY, lrun = 0.f;
    f32x4 O1 = {0.f, 0.f, 0.f, 0.f}, O2 = {0.f, 0.f, 0.f, 0.f};
    u32x4 Kb[4][4], Vb[4][4];
    RAWBAR();
    __builtin_amdgcn_s_sleep(SLEEP0);
    SB();
    ISSUE(0, 0);
    RAWBAR(); SB();
    ISSUE_V(1, 1);
    RAWBAR(); SB();
    RAWBAR(); SB();
    ISSUE_K(1, 1); ISSUE(2, 2); ISSUE(3, 3);
    wg_barrier();
    u32x4 Qf[4];
#pragma unroll
    for (int u = 0; u < 4; ++u) Qf[u] = *(LAS u32x4*)(L + L_Q + ((lane & 3) * 128 + 32 * u + 8 * g) * 2);
    const int jx = c16 >> 2;
    const u32 m0k = jx == 0 ? 0x0000FFFFu : jx == 1 ? 0xFFFF0000u : 0u;
    const u32 m1k = jx == 2 ? 0x0000FFFFu : jx == 3 ? 0xFFFF0000u : 0u;
#define PROCESS(slot) do { \
    f32x4 s_ = {0.f, 0.f, 0.f, 0.f}; \
    _Pragma("unroll") for (int u = 0; u < 4; ++u) s_ = mfma32(Kb[slot][u], Qf[u], s_); \
    const float tm_ = xrow_max(fmaxf(fmaxf(s_[0], s_[1]), fmaxf(s_[2], s_[3]))); \
    if (__any(tm_ > mrun)) { \
      const float mn_ = fmaxf(mrun, tm_), al_ = __builtin_amdgcn_exp2f((mrun - mn_) * SC_LOG2); \
      lrun *= al_; mrun = mn_; \
      const float a0_ = dpp<0x00>(al_), a1_ = dpp<0x55>(al_), a2_ = dpp<0xAA>(al_), a3_ = dpp<0xFF>(al_); \
      O1[0] *= a0_; O1[1] *= a1_; O1[2] *= a2_; O1[3] *= a3_; O2[0] *= a0_; O2[1] *= a1_; O2[2] *= a2_; O2[3] *= a3_; \
    } \
    const float mc_ = mrun * SC_LOG2; \
    _Pragma("unroll") for (int i = 0; i < 4; ++i) { \
      const float p_ = __builtin_amdgcn_exp2f(fmaf(s_[i], SC_LOG2, -mc_)); lrun += p_; \
      const u32 pv_ = cvtpk(p_, p_); const u32x2 a_ = {pv_ & m0k, pv_ & m1k}; \
      O1 = mfma16(a_, u32x2{Vb[slot][i].x, Vb[slot][i].y}, O1); O2 = mfma16(a_, u32x2{Vb[slot][i].z, Vb[slot][i].w}, O2); \
    } SB(); } while (0)
#pragma unroll 1
    for (int it = 0; it < 28; it += 4) {
      PROCESS(0); PACE(it + 4); ISSUE(0, it + 4); PROCESS(1); PACE(it + 5); ISSUE(1, it + 5);
      PROCESS(2); PACE(it + 6); ISSUE(2, it + 6); PROCESS(3); PACE(it + 7); ISSUE(3, it + 7);
    }
    PROCESS(0); ISSUE(0, 32); PROCESS(1);
    if (sw < nextra) PROCESS(2);
    if (4 + sw < nextra) PROCESS(3);
    if (8 + sw < nextra) PROCESS(0);
    asm volatile("s_waitcnt lgkmcnt(0)" : "+s"(E));
    T64 = E + 1;
    lrun = xrow_sum(lrun);
#pragma unroll
    for (int h = 0; h < 4; ++h) *(LAS f32x2*)(L + L_MRG + ((sw * 4 + h) * 64 + lane) * 8) = f32x2{O1[h], O2[h]};
    if (lane < 4) { *(LAS float*)(L + L_ST + (lane * 4 + sw) * 4) = mrun; *(LAS float*)(L + L_ST + 64 + (lane * 4 + sw) * 4) = lrun; }
    wg_barrier();
  }
  const u32 T32 = (u32)T64;

  if (w == 1) { __builtin_amdgcn_s_sleep(E1SLEEP); if (lane == 0) __hip_atomic_store((GAS u64*)(wsg + WS_H1E + (kvh * 32 + j) * FS), T64, RLX); }
  if (w == 0) {
    const int h = g;
    const f32x4 ma = *(LAS f32x4*)(L + L_ST + h * 16), la = *(LAS f32x4*)(L + L_ST + 64 + h * 16);
    const float M = fmaxf(fmaxf(ma[0], ma[1]), fmaxf(ma[2], ma[3]));
    float Ls = 0.f;
    f32x4 oa = {0.f, 0.f, 0.f, 0.f}, ob = {0.f, 0.f, 0.f, 0.f};
#pragma unroll
    for (int ww = 0; ww < 4; ++ww) {
      const float f = __builtin_amdgcn_exp2f((ma[ww] - M) * SC_LOG2);
      Ls = fmaf(f, la[ww], Ls);
      oa += f * *(LAS f32x4*)(L + L_MRG + ((ww * 4 + h) * 64 + 4 * c16) * 8);
      ob += f * *(LAS f32x4*)(L + L_MRG + ((ww * 4 + h) * 64 + 4 * c16) * 8 + 16);
    }
    const float inv = __builtin_amdgcn_rcpf(Ls);
    const u32x4 pk = {nosent(cvtpk(oa[0] * inv, oa[1] * inv)), nosent(cvtpk(oa[2] * inv, oa[3] * inv)), nosent(cvtpk(ob[0] * inv, ob[1] * inv)), nosent(cvtpk(ob[2] * inv, ob[3] * inv))};
    __builtin_amdgcn_raw_buffer_store_b128(pk, rws, WS_PART + (kvh * 32 + j) * 1024 + lane * 16, 0, AUX_SC1);
    const float lam = fmaf(M, SC_LOG2, __builtin_amdgcn_logf(Ls));
    if (c16 == 0) __hip_atomic_store((GAS u64*)(wsg + WS_H1F + (kvh * 32 + j) * FS) + h, ((u64)T32 << 32) | f2u(lam), RLX);
  }
  if (role >= 2) {
    if (tid == 0) *(LAS u32*)(L + L_CNT + 8) = 0u;
    SB(); RAWBAR(); SB();
    if (w < 4) { u32x4 wst[16]; LOAD_WEIGHTS(wst); STAGE_WEIGHTS(wst); }
    wg_barrier();
  }

  if (role == 1) {
    u32x4 Bf[16];
    if (w < 4) {
      const int q = (lane >> 2) & 3;
#pragma unroll
      for (int k = 0; k < 16; ++k) {
        const int d0 = (k >> 2) * 128 + 32 * g + 8 * (q >> 1) + 4 * (q & 1) + (k & 3), d1 = d0 + 16;
        const int sg = (q >> 1) | ((g & 1) << 1);
        const u32x2 lo = tr_read(L + L_ROLE + (d0 ^ (q & 1)) * 128 + 32 * (w ^ sg) + (lane & 3) * 8);
        const u32x2 hi = tr_read(L + L_ROLE + (d1 ^ (q & 1)) * 128 + 32 * (w ^ sg) + (lane & 3) * 8);
        Bf[k] = u32x4{lo.x, lo.y, hi.x, hi.y};
      }
    } else {
#pragma unroll
      for (int k = 0; k < 16; ++k) Bf[k] = u32x4{0u, 0u, 0u, 0u};
    }
    GAS u64* fp = (GAS u64*)(wsg + WS_H1F + (kvh * 32 + 4 * w) * FS) + g;
    const int po = WS_PART + (kvh * 32 + 4 * w) * 1024 + lane * 16;
    LAS unsigned char* pl = L + L_P + w * P_STR + lane * 16;
    LAS unsigned char* al = L + L_P + (lane & 7) * P_STR + g * 16;
    int spins = 0;
    {
      GAS u64* ep = (GAS u64*)(wsg + WS_H1E + (kvh * 32 + 4 * w + (lane & 3)) * FS);
      for (;;) {
        if (__all(__hip_atomic_load(ep, RLX) == T64) || ++spins > SPIN_MAX) break;
        MEMFENCE(); __builtin_amdgcn_s_sleep(1);
      }
    }
    MEMFENCE();
    spins = 0;
    for (;;) {
      const u64 f0 = __hip_atomic_load(fp, RLX), f1 = __hip_atomic_load(fp + FS / 8, RLX), f2 = __hip_atomic_load(fp + 2 * FS / 8, RLX), f3 = __hip_atomic_load(fp + 3 * FS / 8, RLX);
      u32x4 pv[4];
#pragma unroll
      for (int i = 0; i < 4; ++i) pv[i] = LDB128(rws, po + i * 1024, 0, AUX_SC1);
      SB();
      const float l0 = u2f((u32)f0), l1 = u2f((u32)f1), l2 = u2f((u32)f2), l3 = u2f((u32)f3);
      const float lm = fmaxf(fmaxf(l0, l1), fmaxf(l2, l3));
      float e[4] = {__builtin_amdgcn_exp2f(l0 - lm), __builtin_amdgcn_exp2f(l1 - lm), __builtin_amdgcn_exp2f(l2 - lm), __builtin_amdgcn_exp2f(l3 - lm)};
      const float inv = __builtin_amdgcn_rcpf((e[0] + e[1]) + (e[2] + e[3]));
#pragma unroll
      for (int i = 0; i < 4; ++i) e[i] *= inv;
      SB();
      u32 m = 0; f32x2 o[4];
#pragma unroll
      for (int d = 0; d < 4; ++d) o[d] = f32x2{0.f, 0.f};
#pragma unroll
      for (int i = 0; i < 4; ++i) {
        m = mx4(m, pv[i]);
#pragma unroll
        for (int d = 0; d < 4; ++d) o[d] += e[i] * f32x2{bflo(pv[i][d]), bfhi(pv[i][d])};
      }
      *(LAS u32x4*)pl = u32x4{cvtpk(o[0][0], o[0][1]), cvtpk(o[1][0], o[1][1]), cvtpk(o[2][0], o[2][1]), cvtpk(o[3][0], o[3][1])};
      const bool ok = (m != SENT) & ((u32)(f0 >> 32) == T32) & ((u32)(f1 >> 32) == T32) & ((u32)(f2 >> 32) == T32) & ((u32)(f3 >> 32) == T32);
      if (__all(ok) || ++spins > SPIN_MAX) break;
      MEMFENCE();
    }
    wg_barrier();
    if (w == 1) { __builtin_amdgcn_s_sleep(E2SLEEP); if (lane < 8) __hip_atomic_store((GAS u64*)(wsg + WS_H2F + (lane * 64 + kvh * 8 + b) * FS), T64, RLX); }
    if (w < 4) {
      u32x4 av[16];
#pragma unroll
      for (int k = 0; k < 16; ++k) av[k] = *(LAS u32x4*)(al + k * 64);
      SB();
      f32x4 a0 = {0.f, 0.f, 0.f, 0.f}, a1 = {0.f, 0.f, 0.f, 0.f};
#pragma unroll
      for (int k = 0; k < 16; k += 2) { a0 = mfma32(av[k], Bf[k], a0); a1 = mfma32(av[k + 1], Bf[k + 1], a1); }
      const float yv4[4] = {a0[0] + a1[0], a0[1] + a1[1], a0[2] + a1[2], a0[3] + a1[3]};
      const bool odd = lane & 1;
      const u32x2 pk = pack_pairs(yv4, odd);
      const int yo = WS_Y + ((kvh * 8 + 4 * g + (odd ? 2 : 0)) * DM + 64 * b + 16 * w + (c16 & 14)) * 2;
      if (lane < 32) { __builtin_amdgcn_raw_buffer_store_b32(pk.x, rws, yo, 0, AUX_SC1); __builtin_amdgcn_raw_buffer_store_b32(pk.y, rws, yo + DM * 2, 0, AUX_SC1); }
    }
  } else if (role == 2) {
    u32x4 Bf[16];
    if (w < 4) {
      const int q = (lane >> 2) & 3, p = lane & 3;
#pragma unroll
      for (int k = 0; k < 16; ++k) {
        LAS unsigned char* pp = L + L_ROLE + (p >> 1) * 32768 + (32 * k + 8 * g + q) * 64 + 32 * ((w >> 1) ^ (g & 1)) + 16 * (w & 1) + 8 * (p & 1);
        const u32x2 lo = tr_read(pp), hi = tr_read(pp + 256);
        Bf[k] = u32x4{lo.x, lo.y, hi.x, hi.y};
      }
    } else {
#pragma unroll
      for (int k = 0; k < 16; ++k) Bf[k] = u32x4{0u, 0u, 0u, 0u};
    }
    const u32x4 xv = LDB128(rx, (w * DM + 8 * lane) * 2, 0, 0);
    float x8[8];
#pragma unroll
    for (int i = 0; i < 4; ++i) { x8[2 * i] = bflo(xv[i]); x8[2 * i + 1] = bfhi(xv[i]); }
    const int yo = WS_Y + (w * DM + 8 * lane) * 2;
    LAS unsigned char* aw = L + L_A + w * A_STR + lane * 16;
    LAS unsigned char* ar = L + L_A + (lane & 7) * A_STR + 8 * g * 2;
    u32x4 yv[8];
    {
      GAS u64* fp = (GAS u64*)(wsg + WS_H2F + (kvh * 64 + lane) * FS);
      LAS u32* mk = (LAS u32*)(L + L_CNT + 8);
      u32 done = 0; int spins = 0;
#pragma unroll
      for (int i = 0; i < 8; ++i) yv[i] = u32x4{0u, 0u, 0u, 0u};
      for (;;) {
        u32 rdy;
        if (w == 0) {
          const u64 bal = __builtin_amdgcn_ballot_w64(__hip_atomic_load(fp, RLX) == T64);
          rdy = 0;
#pragma unroll
          for (int i = 0; i < 8; ++i) rdy |= (((bal >> (8 * i)) & 0xFFull) == 0xFFull ? 1u : 0u) << i;
          if (rdy != done && lane == 0) __hip_atomic_store(mk, rdy, __ATOMIC_RELAXED, __HIP_MEMORY_SCOPE_WORKGROUP);
        } else {
          rdy = __builtin_amdgcn_readfirstlane(__hip_atomic_load(mk, __ATOMIC_RELAXED, __HIP_MEMORY_SCOPE_WORKGROUP));
        }
#pragma unroll
        for (int i = 0; i < 8; ++i)
          if (((rdy & ~done) >> i) & 1u) yv[i] = LDB128(rws, yo + i * (8 * DM * 2), 0, AUX_SC1);
        done |= rdy;
        if (done == 0xFFu || ++spins > SPIN_MAX * 8) break;
        MEMFENCE(); __builtin_amdgcn_s_sleep(1);
      }
    }
    {
      int spins = 0;
      for (;;) {
        u32 bad = 0;
#pragma unroll
        for (int i = 0; i < 8; ++i) if (!__all(mx4(0u, yv[i]) != SENT)) bad |= 1u << i;
        if (bad == 0 || ++spins > SPIN_MAX) break;
        MEMFENCE();
#pragma unroll
        for (int i = 0; i < 8; ++i) if ((bad >> i) & 1u) yv[i] = LDB128(rws, yo + i * (8 * DM * 2), 0, AUX_SC1);
      }
    }
    float v[8];
#pragma unroll
    for (int e = 0; e < 8; ++e) v[e] = x8[e];
#pragma unroll
    for (int i = 0; i < 8; ++i) {
#pragma unroll
      for (int e = 0; e < 4; ++e) { v[2 * e] += bflo(yv[i][e]); v[2 * e + 1] += bfhi(yv[i][e]); }
    }
    *(LAS u32x4*)aw = ln_row(v);
    wg_barrier();
    if (w == 1) { __builtin_amdgcn_s_sleep(E3SLEEP); if (lane < 8) __hip_atomic_store((GAS u64*)(wsg + WS_H3F + (lane * 64 + cgu) * FS), T64, RLX); }
    if (w < 4) {
      u32x4 av[16];
#pragma unroll
      for (int k = 0; k < 16; ++k) av[k] = *(LAS u32x4*)(ar + k * 64);
      SB();
      f32x4 a0 = {0.f, 0.f, 0.f, 0.f}, a1 = {0.f, 0.f, 0.f, 0.f};
#pragma unroll
      for (int k = 0; k < 16; k += 2) { a0 = mfma32(av[k], Bf[k], a0); a1 = mfma32(av[k + 1], Bf[k + 1], a1); }
      float r4[4];
#pragma unroll
      for (int e = 0; e < 4; ++e) {
        const float z = a0[e] + a1[e];
        const float up = dpp<0x128>(z);
        const float t = z * fmaf(z * z, 0.044715f * 0.7978845608028654f, 0.7978845608028654f);
        const float sg = __builtin_amdgcn_rcpf(1.0f + __builtin_amdgcn_exp2f(t * (-2.0f * 1.4426950408889634f)));
        r4[e] = z * sg * up;
      }
      const bool odd = lane & 1;
      const u32x2 pk = pack_pairs(r4, odd);
      const int gof = WS_G + ((4 * g + (odd ? 2 : 0)) * FF + 32 * cgu + 8 * w + (c16 & 6)) * 2;
      if (lane < 32 && c16 < 8) { __builtin_amdgcn_raw_buffer_store_b32(pk.x, rws, gof, 0, AUX_SC1); __builtin_amdgcn_raw_buffer_store_b32(pk.y, rws, gof + FF * 2, 0, AUX_SC1); }
    }
  } else if (role == 3) {
    u32x4 Bf[7];
#pragma unroll
    for (int u = 0; u < 7; ++u) Bf[u] = bfrag(L + L_ROLE + (224 * w + 32 * u) * 32 + trl, 128);
    int go[4]; LAS unsigned char* gl[4];
#pragma unroll
    for (int i = 0; i < 4; ++i) {
      const int ci = (w + 8 * i) * 64 + lane, row = ci / 224; const bool in = i < 3 || w < 4;
      go[i] = in ? WS_G + ci * 16 : WS_END;
      gl[i] = L + L_P + (in ? row * G_STR + (ci - row * 224) * 16 : 8 * G_STR + lane * 16);
    }
    LAS unsigned char* ar = L + L_P + (lane & 7) * G_STR + (7 * w * 32 + 8 * g) * 2;
    LAS unsigned char* rw = L + L_RED + ((w * 8 + 4 * g) * 16 + c16) * 4;
    f32x2 hres = {0.f, 0.f};
    if (w == 0) {
      const u32 xw = *(const GAS u32*)((const GAS unsigned char*)x + (rb * DM + 16 * cdn + 2 * cp) * 2);
      GAS u64* fp = (GAS u64*)(wsg + WS_H2F + (kvh * 64 + (lane & 7) * 8 + (cdn >> 2)) * FS); int spins = 0;
      for (;;) { const u64 f0 = __hip_atomic_load(fp, RLX); if (__all(f0 == T64) || ++spins > SPIN_MAX) break; MEMFENCE(); __builtin_amdgcn_s_sleep(2); }
      MEMFENCE();
      for (spins = 0;; ) {
        u32 yy[8], m = 0;
#pragma unroll
        for (int q = 0; q < 8; ++q) yy[q] = __builtin_amdgcn_raw_buffer_load_b32(rws, WS_Y + ((q * 8 + rb) * DM + 16 * cdn + 2 * cp) * 2, 0, AUX_SC1);
        hres = f32x2{bflo(xw), bfhi(xw)};
#pragma unroll
        for (int q = 0; q < 8; ++q) { m = umax(m, yy[q]); hres += f32x2{bflo(yy[q]), bfhi(yy[q])}; }
        if (__all(m != SENT) || ++spins > SPIN_MAX) break;
        MEMFENCE();
      }
      GAS u64* gp = (GAS u64*)(wsg + WS_H3F + (kvh * 64 + lane) * FS); spins = 0;
      for (;;) {
        const u64 f0 = lane < NGU ? __hip_atomic_load(gp, RLX) : T64;
        if (__all(f0 == T64) || ++spins > SPIN_MAX) break;
        MEMFENCE(); __builtin_amdgcn_s_sleep(1);
      }
      if (cdn == 0 && lane == 0) __hip_atomic_store((GAS u64*)(wsg + WS_E), T64, RLX);
    }
    wg_barrier();
    for (int spins = 0;; ) {
      u32x4 gv[4];
#pragma unroll
      for (int i = 0; i < 4; ++i) gv[i] = LDB128(rws, go[i], 0, AUX_SC1);
      SB();
      u32 m = 0;
#pragma unroll
      for (int i = 0; i < 4; ++i) { m = mx4(m, gv[i]); *(LAS u32x4*)gl[i] = gv[i]; }
      if (__all(m != SENT) || ++spins > SPIN_MAX) break;
      MEMFENCE();
    }
    wg_barrier();
    {
      u32x4 av[7];
#pragma unroll
      for (int u = 0; u < 7; ++u) av[u] = *(LAS u32x4*)(ar + u * 64);
      SB();
      f32x4 a0 = {0.f, 0.f, 0.f, 0.f}, a1 = {0.f, 0.f, 0.f, 0.f};
#pragma unroll
      for (int u = 0; u < 7; ++u) { if (u & 1) a1 = mfma32(av[u], Bf[u], a1); else a0 = mfma32(av[u], Bf[u], a0); }
      if (lane < 32) {
#pragma unroll
        for (int r = 0; r < 4; ++r) *(LAS float*)(rw + r * 64) = a0[r] + a1[r];
      }
    }
    wg_barrier();
    if (w == 0) {
      f32x2 rr[8];
#pragma unroll
      for (int ww = 0; ww < 8; ++ww) rr[ww] = *(LAS f32x2*)(L + L_RED + ((ww * 8 + rb) * 16 + 2 * cp) * 4);
      const f32x2 sm = hres + (((rr[0] + rr[1]) + (rr[2] + rr[3])) + ((rr[4] + rr[5]) + (rr[6] + rr[7])));
      *(GAS u32*)((GAS unsigned char*)out + (rb * DM + 16 * cdn + 2 * cp) * 2) = cvtpk(sm[0], sm[1]);
    }
  }
}

extern "C" void kernel_launch(void* const* d_in, const int* in_sizes, int n_in, void* d_out, int out_size, void* d_ws, size_t ws_size, hipStream_t stream) {
  fused_decode_block<<<NWG, NTHR, 0, stream>>>((const u16*)d_in[0], (const u16*)d_in[1], (const u16*)d_in[2], (const u16*)d_in[3], (const u16*)d_in[4],
                                              (const u16*)d_in[6], (const u16*)d_in[7], (const u16*)d_in[8], (u16*)d_out, (unsigned char*)d_ws);
}
```
